```python
import torch

def _softmax(x, axis=-1):
    return torch.softmax(x, dim=axis)
def _gelu(x):
    return torch.nn.functional.gelu(x, approximate="tanh")
def _silu(x):
    return torch.nn.functional.silu(x)
def _sigmoid(x):
    return torch.sigmoid(x)
def _layernorm(x, axis=-1, eps=1e-5):
    m = x.mean(dim=axis, keepdim=True)
    v = x.var(dim=axis, keepdim=True, unbiased=False)
    return (x - m) / torch.sqrt(v + eps)
def _scatter_add(tgt, idx, src, axis=0):
    return tgt.clone().index_add_(axis, idx, src)
def _segment_sum(src, seg, n):
    out = torch.zeros((n,) + src.shape[1:], dtype=src.dtype)
    return out.index_add_(0, seg, src)
def _pad(x, pw):
    flat = tuple(v for pair in reversed(pw) for v in pair)
    return torch.nn.functional.pad(x, flat)

kernel_name = 'parallel_sum__mha_block__glu_gated_swiglu__prompt'

def setup_inputs():

    _w = (lambda a: (lambda t: t.to(torch.bfloat16) if t.is_floating_point() else t)(torch.as_tensor(a)))
    def _A_setup():
        B, S, D, H = 1, 2048, 512, 32
        return {
            "x": _w((lambda *s: torch.randn(*s, dtype=torch.bfloat16) * 0.05)(B, S, D)),
            "wqkv": _w((lambda *s: torch.randn(*s, dtype=torch.bfloat16) * 0.05)(D, 3 * D)),
            "wo": _w((lambda *s: torch.randn(*s, dtype=torch.bfloat16) * 0.05)(D, D)),
        }
    _A_in = _A_setup()

    def _B_setup():
        B, S, D, F = 1, 2048, 512, 1792
        return {
            "x": _w((lambda *s: torch.randn(*s, dtype=torch.bfloat16) * 0.05)(B, S, D)),
            "w1": _w((lambda *s: torch.randn(*s, dtype=torch.bfloat16) * 0.4)(D, F) * (2.0 / D) ** 0.5),
            "w3": _w((lambda *s: torch.randn(*s, dtype=torch.bfloat16) * 0.4)(D, F) * (2.0 / D) ** 0.5),
            "w2": _w((lambda *s: torch.randn(*s, dtype=torch.bfloat16) * 0.4)(F, D) * (2.0 / F) ** 0.5),
        }
    _B_in = _B_setup()

    return {
        "A_x": _A_in["x"],
        "A_wqkv": _A_in["wqkv"],
        "A_wo": _A_in["wo"],
        "B_x": _B_in["x"],
        "B_w1": _B_in["w1"],
        "B_w3": _B_in["w3"],
        "B_w2": _B_in["w2"],
    }

def reference(A_x, A_wqkv, A_wo, B_x, B_w1, B_w3, B_w2):
    def _A_ref(x, wqkv, wo):
        B, S, D = x.shape
        H = 32
        Dh = D // H
        qkv = (lambda x, *d: x.permute(*d))((x @ wqkv).reshape(B, S, 3, H, Dh), 2, 0, 3, 1, 4)
        q, k, v = qkv[0], qkv[1], qkv[2]
        scores = torch.einsum("bhqd,bhkd->bhqk", q, k) * (Dh ** -0.5)
        p = _softmax(scores, axis=-1)
        o = torch.einsum("bhqk,bhkd->bhqd", p, v)
        return (lambda x, *d: x.permute(*d))(o, 0, 2, 1, 3).reshape(B, S, D) @ wo

    def _B_ref(x, w1, w3, w2):
        gate = torch.einsum("bsd,df->bsf", x, w1)
        up = torch.einsum("bsd,df->bsf", x, w3)
        h = _silu(gate) * up
        out = torch.einsum("bsf,fd->bsd", h, w2)
        return out

    x = A_x
    xn = _layernorm(x, axis=-1)
    return x + _A_ref(xn, A_wqkv, A_wo) + _B_ref(xn, B_w1, B_w3, B_w2)
```

```cpp
#include <hip/hip_runtime.h>
#include <stdint.h>

typedef __attribute__((ext_vector_type(8))) __bf16 bf16x8;
typedef __attribute__((ext_vector_type(4))) __bf16 bf16x4;
typedef __attribute__((ext_vector_type(2))) __bf16 bf16x2;
typedef __attribute__((ext_vector_type(16))) float f32x16;
typedef __attribute__((ext_vector_type(2))) float f32x2;
typedef __attribute__((ext_vector_type(4))) uint32_t u32x4;
typedef __attribute__((ext_vector_type(2))) uint32_t u32x2;
typedef __attribute__((address_space(1))) const void* gptr_t;
typedef __attribute__((address_space(3))) void* lptr_t;
typedef __attribute__((address_space(3))) bf16x4* ltr_t;
#define DEV __device__ __forceinline__

constexpr size_t WS_QF = 0;
constexpr size_t WS_KF = 2u << 20;
constexpr size_t WS_VF = 4u << 20;
constexpr size_t WS_XN = 6u << 20;
constexpr size_t WS_OF = 8u << 20;
constexpr size_t WS_HF = 10u << 20;
constexpr size_t WS_NR = 17u << 20;

DEV void glds16(const void* g, char* l) {
  __builtin_amdgcn_global_load_lds((gptr_t)g, (lptr_t)l, 16, 0, 0);
}
DEV bf16x8 lds128(const char* p) { return *(const bf16x8*)p; }
DEV bf16x8 ldstr2(const char* p, int off0, int off1) {
  bf16x4 a = __builtin_amdgcn_ds_read_tr16_b64_v4bf16((ltr_t)(p + off0));
  bf16x4 b = __builtin_amdgcn_ds_read_tr16_b64_v4bf16((ltr_t)(p + off1));
  return __builtin_shufflevector(a, b, 0, 1, 2, 3, 4, 5, 6, 7);
}
DEV f32x16 mfma(bf16x8 a, bf16x8 b, f32x16 c) { return __builtin_amdgcn_mfma_f32_32x32x16_bf16(a, b, c, 0, 0, 0); }
DEV uint32_t pk(float a, float b) { f32x2 v = {a, b}; bf16x2 r = __builtin_convertvector(v, bf16x2); return __builtin_bit_cast(uint32_t, r); }
DEV u32x4 pk8(const f32x16& a, int o) { u32x4 r = {pk(a[o], a[o + 1]), pk(a[o + 2], a[o + 3]), pk(a[o + 4], a[o + 5]), pk(a[o + 6], a[o + 7])}; return r; }
DEV float bflo(uint32_t u) { return __builtin_bit_cast(float, u << 16); }
DEV float bfhi(uint32_t u) { return __builtin_bit_cast(float, u & 0xffff0000u); }
DEV float dot2(uint32_t a, uint32_t b, float c) { return __builtin_amdgcn_fdot2_f32_bf16(__builtin_bit_cast(bf16x2, a), __builtin_bit_cast(bf16x2, b), c, false); }
template <int C> DEV float dppf(float v) { return __builtin_bit_cast(float, __builtin_amdgcn_update_dpp(0, __builtin_bit_cast(int, v), C, 0xf, 0xf, true)); }
DEV float row16_sum(float v) { v += dppf<0x128>(v); v += dppf<0x124>(v); v += dppf<0x122>(v); v += dppf<0x121>(v); return v; }
DEV float row16_max(float v) { v = fmaxf(v, dppf<0x128>(v)); v = fmaxf(v, dppf<0x124>(v)); v = fmaxf(v, dppf<0x122>(v)); v = fmaxf(v, dppf<0x121>(v)); return v; }
DEV float half_sum(float v) {
  uint32_t u = __builtin_bit_cast(uint32_t, v);
  auto r = __builtin_amdgcn_permlane32_swap(u, u, false, false);
  return __builtin_bit_cast(float, (uint32_t)r[0]) + __builtin_bit_cast(float, (uint32_t)r[1]);
}
DEV float half_max(float v) {
  uint32_t u = __builtin_bit_cast(uint32_t, v);
  auto r = __builtin_amdgcn_permlane32_swap(u, u, false, false);
  return fmaxf(__builtin_bit_cast(float, (uint32_t)r[0]), __builtin_bit_cast(float, (uint32_t)r[1]));
}
DEV float rdlane(float v, int l) { return __builtin_bit_cast(float, __builtin_amdgcn_readlane(__builtin_bit_cast(int, v), l)); }
#define WAITV(n) asm volatile("s_waitcnt vmcnt(" #n ")" ::: "memory")
#define BAR() do { asm volatile("s_waitcnt lgkmcnt(0)" ::: "memory"); __builtin_amdgcn_s_barrier(); asm volatile("" ::: "memory"); } while (0)

constexpr int K1_W = 65536, K1_ST = 24576;
__global__ __launch_bounds__(512) void k1_qkv(const uint16_t* __restrict__ x, const uint16_t* __restrict__ wqkv, char* __restrict__ ws) {
  __shared__ __attribute__((aligned(1024))) char sm[65536 + 3 * K1_ST];
  const int tid = threadIdx.x, lane = tid & 63, w = __builtin_amdgcn_readfirstlane(tid >> 6);
  const int b = blockIdx.x, xcd = b & 7, idx = b >> 3;
  const int tt = (xcd & 3) * 8 + (idx & 7), g = (xcd >> 2) * 4 + (idx >> 3);
#pragma unroll
  for (int i = 0; i < 8; ++i) {
    const int r = 8 * w + i;
    glds16(x + (size_t)(tt * 64 + r) * 512 + ((lane ^ (r & 15)) << 3), sm + r * 1024);
  }
  const int wrow = lane >> 3, wch = (lane & 7) ^ (((wrow >> 1) & 1) << 2);
  const uint16_t* wsrc = wqkv + (size_t)(8 * w + wrow) * 1536 + g * 64 + wch * 8;
  auto loadW = [&](int st, int buf) {
#pragma unroll
    for (int s = 0; s < 3; ++s) glds16(wsrc + (size_t)st * 64 * 1536 + s * 512, sm + K1_W + buf * K1_ST + s * 8192 + w * 1024);
  };
  loadW(0, 0); loadW(1, 1);
  WAITV(6);
#pragma unroll
  for (int p = 0; p < 2; ++p) {
    char* rp = sm + (8 * w + 4 * p + (lane >> 4)) * 1024 + (lane & 15) * 16;
    u32x4 v[4];
#pragma unroll
    for (int q = 0; q < 4; ++q) v[q] = *(const u32x4*)(rp + q * 256);
    float s = 0.f, ss = 0.f;
#pragma unroll
    for (int q = 0; q < 4; ++q)
#pragma unroll
      for (int e = 0; e < 4; ++e) { s = dot2(v[q][e], 0x3f803f80u, s); ss = dot2(v[q][e], v[q][e], ss); }
    s = row16_sum(s); ss = row16_sum(ss);
    const float mean = s * (1.f / 512.f);
    const float var = fmaxf(ss * (1.f / 512.f) - mean * mean, 0.f);
    const float rstd = 1.0f / sqrtf(var + 1e-5f), nb = -mean * rstd;
#pragma unroll
    for (int q = 0; q < 4; ++q) {
      u32x4 o;
#pragma unroll
      for (int e = 0; e < 4; ++e) o[e] = pk(fmaf(bflo(v[q][e]), rstd, nb), fmaf(bfhi(v[q][e]), rstd, nb));
      *(u32x4*)(rp + q * 256) = o;
    }
  }
  const int h = lane >> 5, r31 = lane & 31;
  const int xb = r31 * 1024 + ((h ^ (r31 & 15)) << 4);
  const int q4 = (lane & 15) >> 2, p4 = lane & 3, blk = (lane >> 4) & 1;
  const int ct = w < 4 ? w : 4 + (w & 1);
  const int seg = ct >> 1, c = ct & 1;
  const int wb = K1_W + seg * 8192 + (8 * h + q4) * 128 + (((4 * c + 2 * blk + (p4 >> 1)) ^ ((q4 >> 1) << 2)) << 4) + 8 * (p4 & 1);
  const int vt = (w >> 1) & 1;
  f32x16 a0 = {}, a1 = {};
#pragma unroll
  for (int st = 0; st < 8; ++st) {
    if (st < 7) WAITV(3); else WAITV(0);
    BAR();
    if (st < 6) loadW(st + 2, (st + 2) % 3);
    const char* wp = sm + wb + (st % 3) * K1_ST;
#pragma unroll
    for (int kk = 0; kk < 4; ++kk) {
      const int ks = st * 4 + kk;
      const char* xp = sm + ((xb ^ ((ks & 7) << 5)) + (ks >> 3) * 256);
      bf16x8 wf = ldstr2(wp, kk * 2048, kk * 2048 + 512);
      if (w < 4) {
        a0 = mfma(wf, lds128(xp), a0);
        a1 = mfma(wf, lds128(xp + 32768), a1);
      } else {
        a0 = mfma(lds128(xp + vt * 32768), wf, a0);
      }
    }
  }
  float* nr = (float*)(ws + WS_NR);
  if (w < 4) {
    const float sc = w < 2 ? 0.25f * 1.4426950408889634f : 1.f;
    char* dst = ws + (w < 2 ? WS_QF : WS_KF);
#pragma unroll
    for (int t = 0; t < 2; ++t) {
      f32x16 a = t ? a1 : a0;
#pragma unroll
      for (int hh = 0; hh < 2; ++hh) {
        float n2 = 0.f;
#pragma unroll
        for (int j = 0; j < 8; ++j) { a[8 * hh + j] *= sc; n2 = fmaf(a[8 * hh + j], a[8 * hh + j], n2); }
        const int head = 4 * g + 2 * c + hh, tile = 2 * tt + t;
        *(u32x4*)(dst + (size_t)(head * 64 + tile) * 1024 + lane * 16) = pk8(a, 8 * hh);
        n2 = row16_max(half_sum(n2));
        n2 = fmaxf(rdlane(n2, 0), rdlane(n2, 16));
        if (lane == 0) nr[(seg * 32 + head) * 64 + tile] = n2;
      }
    }
  } else {
    const int head = 4 * g + 2 * c + (r31 >> 4), tile = 2 * tt + vt;
    char* dst = ws + WS_VF + (size_t)(head * 64 + tile) * 1024 + h * 256 + (r31 & 15) * 16;
    *(u32x4*)(dst) = pk8(a0, 0);
    *(u32x4*)(dst + 512) = pk8(a0, 8);
    float m = 0.f;
#pragma unroll
    for (int j = 0; j < 16; ++j) m = fmaxf(m, fabsf(a0[j]));
    m = half_max(row16_max(m));
    if ((lane & 47) == 0) nr[(64 + head) * 64 + tile] = m;
  }
  {
    const int t = w >> 2, ks = 4 * g + (w & 3);
    const u32x4 v = *(const u32x4*)(sm + t * 32768 + ((xb ^ ((ks & 7) << 5)) + (ks >> 3) * 256));
    *(u32x4*)(ws + WS_XN + (size_t)((2 * tt + t) * 32 + ks) * 1024 + lane * 16) = v;
  }
}

constexpr int AT_ONES = 49152;
__global__ __launch_bounds__(512) void k2_attn(char* __restrict__ ws) {
  __shared__ __attribute__((aligned(1024))) char sm[49152 + 1024];
  const int tid = threadIdx.x, lane = tid & 63, w = __builtin_amdgcn_readfirstlane(tid >> 6);
  const int b = blockIdx.x, head = (b & 7) * 4 + (b >> 6), qg = (b >> 3) & 7;
  const char* kg = ws + WS_KF + (size_t)head * 65536 + w * 1024 + lane * 16;
  const char* vg = ws + WS_VF + (size_t)head * 65536 + w * 1024 + lane * 16;
  auto loadKV = [&](int ch, int buf) {
    glds16(kg + ch * 8192, sm + buf * 16384 + w * 1024);
    glds16(vg + ch * 8192, sm + buf * 16384 + 8192 + w * 1024);
  };
  loadKV(0, 0); loadKV(1, 1);
  const int qt = qg * 8 + w;
  const bf16x8 qf = *(const bf16x8*)(ws + WS_QF + (size_t)(head * 64 + qt) * 1024 + lane * 16);
  if (tid < 256) ((uint32_t*)(sm + AT_ONES))[tid] = 0x3f803f80u;
  const float* nr = (const float*)(ws + WS_NR);
  float kmax = nr[(32 + head) * 64 + lane], vmax = nr[(64 + head) * 64 + lane], qmax = nr[head * 64 + qt];
  kmax = row16_max(kmax); vmax = row16_max(vmax);
  kmax = fmaxf(fmaxf(rdlane(kmax, 0), rdlane(kmax, 16)), fmaxf(rdlane(kmax, 32), rdlane(kmax, 48)));
  vmax = fmaxf(fmaxf(rdlane(vmax, 0), rdlane(vmax, 16)), fmaxf(rdlane(vmax, 32), rdlane(vmax, 48)));
  const bool fast = __builtin_amdgcn_readfirstlane((int)(qmax * kmax * 1.05f <= 3600.f && vmax <= 1e15f));
  const int h = lane >> 5, m = lane & 31;
  const int vlane = h * 256 + (m & 15) * 16;
  const int vstep = m < 16 ? 1024 : 0;
  f32x16 o = {};
  float mrun = -INFINITY;
  for (int ch = 0; ch < 8; ++ch) {
    WAITV(0);
    BAR();
    if (ch < 6) loadKV(ch + 2, (ch + 2) % 3);
    const int buf = ch % 3;
    const char* kp = sm + buf * 16384 + lane * 16;
    int va = m < 16 ? buf * 16384 + 8192 + vlane : AT_ONES + vlane;
    if (fast) {
#pragma unroll
      for (int t = 0; t < 8; ++t) {
        f32x16 s = mfma(lds128(kp + t * 1024), qf, f32x16{});
        const bf16x8 v0 = lds128(sm + va), v1 = lds128(sm + va + 512);
        va += vstep;
#pragma unroll
        for (int j = 0; j < 16; ++j) s[j] = __builtin_amdgcn_exp2f(s[j]);
        o = mfma(v0, __builtin_bit_cast(bf16x8, pk8(s, 0)), o);
        o = mfma(v1, __builtin_bit_cast(bf16x8, pk8(s, 8)), o);
      }
    } else {
#pragma unroll 1
      for (int t = 0; t < 8; ++t) {
        f32x16 s = mfma(lds128(kp + t * 1024), qf, f32x16{});
        const bf16x8 v0 = lds128(sm + va), v1 = lds128(sm + va + 512);
        va += vstep;
        float mx = s[0];
#pragma unroll
        for (int j = 1; j < 16; ++j) mx = fmaxf(mx, s[j]);
        mx = fmaxf(half_max(mx), mrun);
        const float al = __builtin_amdgcn_exp2f(mrun - mx);
        mrun = mx;
#pragma unroll
        for (int j = 0; j < 16; ++j) { s[j] = __builtin_amdgcn_exp2f(s[j] - mx); o[j] *= al; }
        o = mfma(v0, __builtin_bit_cast(bf16x8, pk8(s, 0)), o);
        o = mfma(v1, __builtin_bit_cast(bf16x8, pk8(s, 8)), o);
      }
    }
  }
  const float inv = 1.0f / o[8];
#pragma unroll
  for (int j = 0; j < 8; ++j) o[j] *= inv;
  *(u32x4*)(ws + WS_OF + (size_t)(qt * 32 + head) * 1024 + lane * 16) = pk8(o, 0);
}

constexpr int GU_ST = 24576;
__global__ __launch_bounds__(512) void k3_gateup(const uint16_t* __restrict__ w1, const uint16_t* __restrict__ w3, char* __restrict__ ws) {
  __shared__ __attribute__((aligned(1024))) char sm[3 * GU_ST];
  const int tid = threadIdx.x, lane = tid & 63, w = __builtin_amdgcn_readfirstlane(tid >> 6);
  const int b = blockIdx.x, xcd = b & 7, idx = b >> 3;
  const int tg = (xcd & 3) * 4 + (idx & 3), fg = (xcd >> 2) * 7 + (idx >> 2);
  const char* xsrc = ws + WS_XN + (size_t)((tg * 4 + (w >> 1)) * 32 + (w & 1)) * 1024 + lane * 16;
  const int wrow = lane >> 4, wch = (lane & 15) ^ ((wrow & 3) << 2);
  const size_t woff = (size_t)(4 * w + wrow) * 1792 + fg * 128 + wch * 8;
  auto load = [&](int st, int buf) {
    char* d = sm + buf * GU_ST + w * 1024;
    glds16(xsrc + st * 2048, d);
    glds16(w1 + woff + (size_t)st * 32 * 1792, d + 8192);
    glds16(w3 + woff + (size_t)st * 32 * 1792, d + 16384);
  };
  load(0, 0); load(1, 1);
  const int h = lane >> 5, q4 = (lane & 15) >> 2, p4 = lane & 3, blk = (lane >> 4) & 1;
  const int c = w & 3, tp = w >> 2;
  const int wb = 8192 + (8 * h + q4) * 256 + (((4 * c + 2 * blk + (p4 >> 1)) ^ (q4 << 2)) << 4) + 8 * (p4 & 1);
  const int xb = tp * 4096 + lane * 16;
  f32x16 g0 = {}, g1 = {}, u0 = {}, u1 = {};
#pragma unroll 1
  for (int st0 = 0; st0 < 18; st0 += 3) {
#pragma unroll
    for (int i = 0; i < 3; ++i) {
      const int st = st0 + i;
      if (st < 16) {
        if (st < 15) WAITV(3); else WAITV(0);
        BAR();
        if (st < 14) load(st + 2, (i + 2) % 3);
        const char* bp = sm + i * GU_ST;
#pragma unroll
        for (int kk = 0; kk < 2; ++kk) {
          const bf16x8 x0 = lds128(bp + xb + kk * 1024), x1 = lds128(bp + xb + 2048 + kk * 1024);
          const bf16x8 wg = ldstr2(bp + wb, kk * 4096, kk * 4096 + 1024);
          const bf16x8 wu = ldstr2(bp + wb + 8192, kk * 4096, kk * 4096 + 1024);
          g0 = mfma(wg, x0, g0); g1 = mfma(wg, x1, g1);
          u0 = mfma(wu, x0, u0); u1 = mfma(wu, x1, u1);
        }
      }
    }
  }
#pragma unroll
  for (int t = 0; t < 2; ++t) {
    f32x16 gg = t ? g1 : g0, uu = t ? u1 : u0;
#pragma unroll
    for (int j = 0; j < 16; ++j) {
      const float e = __builtin_amdgcn_exp2f(gg[j] * -1.4426950408889634f);
      gg[j] = gg[j] * __builtin_amdgcn_rcpf(1.f + e) * uu[j];
    }
    char* dst = ws + WS_HF + (size_t)((tg * 4 + tp * 2 + t) * 112 + (fg * 4 + c) * 2) * 1024 + lane * 16;
    *(u32x4*)dst = pk8(gg, 0);
    *(u32x4*)(dst + 1024) = pk8(gg, 8);
  }
}

constexpr int F_ST = 32768;
__global__ __launch_bounds__(512) void k4_final(const uint16_t* __restrict__ x, const uint16_t* __restrict__ wo, const uint16_t* __restrict__ w2,
                                                const char* __restrict__ ws, uint16_t* __restrict__ out) {
  __shared__ __attribute__((aligned(1024))) char sm[3 * F_ST];
  const int tid = threadIdx.x, lane = tid & 63, w = __builtin_amdgcn_readfirstlane(tid >> 6);
  const int b = blockIdx.x, xcd = b & 7, idx = b >> 3;
  const int tg = (xcd & 3) * 8 + (idx & 7), cg = (xcd >> 2) * 4 + (idx >> 3);
  const int wrow = lane >> 3, wch = (lane & 7) ^ (((wrow >> 1) & 1) << 2);
  const size_t woff = (size_t)(16 * w + wrow) * 512 + cg * 64 + wch * 8;
  auto load = [&](int st, int buf) {
    char* d = sm + buf * F_ST;
    const char* a; const uint16_t* wp; int tstr;
    if (st < 4) { a = ws + WS_OF + (size_t)(tg * 2 * 32 + st * 8 + w) * 1024; tstr = 32 * 1024; wp = wo + (size_t)st * 128 * 512; }
    else { a = ws + WS_HF + (size_t)(tg * 2 * 112 + (st - 4) * 8 + w) * 1024; tstr = 112 * 1024; wp = w2 + (size_t)(st - 4) * 128 * 512; }
    glds16(a + lane * 16, d + w * 1024);
    glds16(a + tstr + lane * 16, d + 8192 + w * 1024);
    glds16(wp + woff, d + 16384 + w * 2048);
    glds16(wp + woff + 8 * 512, d + 16384 + w * 2048 + 1024);
  };
  load(0, 0); load(1, 1);
  const int h = lane >> 5, q4 = (lane & 15) >> 2, p4 = lane & 3, blk = (lane >> 4) & 1;
  const int c = w & 1, kq = w >> 1;
  const int wb = 16384 + kq * 4096 + (4 * h + q4) * 128 + (((4 * c + 2 * blk + (p4 >> 1)) ^ ((q4 >> 1) << 2)) << 4) + 8 * (p4 & 1);
  const int xb = kq * 2048 + lane * 16;
  f32x16 a0 = {}, a1 = {};
#pragma unroll 1
  for (int st0 = 0; st0 < 18; st0 += 3) {
#pragma unroll
    for (int i = 0; i < 3; ++i) {
      const int st = st0 + i;
      if (st < 17) WAITV(4); else WAITV(0);
      BAR();
      if (st < 16) load(st + 2, (i + 2) % 3);
      const char* bp = sm + i * F_ST;
#pragma unroll
      for (int kk = 0; kk < 2; ++kk) {
        const bf16x8 wf = ldstr2(bp + wb, kk * 2048, kk * 2048 + 1024);
        a0 = mfma(wf, lds128(bp + xb + kk * 1024), a0);
        a1 = mfma(wf, lds128(bp + xb + 8192 + kk * 1024), a1);
      }
    }
  }
  BAR();
  typedef __attribute__((ext_vector_type(4))) float f32x4;
  f32x4* red = (f32x4*)sm;
#pragma unroll
  for (int t = 0; t < 2; ++t)
#pragma unroll
    for (int i = 0; i < 4; ++i) {
      const f32x16& a = t ? a1 : a0;
      red[(((kq * 2 + c) * 2 + t) * 4 + i) * 64 + lane] = f32x4{a[4 * i], a[4 * i + 1], a[4 * i + 2], a[4 * i + 3]};
    }
  BAR();
  {
    const int c2 = w & 1, t2 = (w >> 1) & 1, ih = w >> 2;
#pragma unroll
    for (int ii = 0; ii < 2; ++ii) {
      const int i = ih * 2 + ii;
      f32x4 s = red[(((0 * 2 + c2) * 2 + t2) * 4 + i) * 64 + lane];
#pragma unroll
      for (int k = 1; k < 4; ++k) s += red[(((k * 2 + c2) * 2 + t2) * 4 + i) * 64 + lane];
      const size_t o = (size_t)(tg * 64 + t2 * 32 + (lane & 31)) * 512 + cg * 64 + c2 * 32 + 8 * i + 4 * h;
      const u32x2 xv = *(const u32x2*)(x + o);
      u32x2 r = {pk(s[0] + bflo(xv[0]), s[1] + bfhi(xv[0])), pk(s[2] + bflo(xv[1]), s[3] + bfhi(xv[1]))};
      *(u32x2*)(out + o) = r;
    }
  }
}

extern "C" void kernel_launch(void* const* d_in, const int* in_sizes, int n_in, void* d_out, int out_size, void* d_ws, size_t ws_size,
                              hipStream_t stream) {
  const uint16_t* x = (const uint16_t*)d_in[0];
  const uint16_t* wqkv = (const uint16_t*)d_in[1];
  const uint16_t* wo = (const uint16_t*)d_in[2];
  const uint16_t* w1 = (const uint16_t*)d_in[4];
  const uint16_t* w3 = (const uint16_t*)d_in[5];
  const uint16_t* w2 = (const uint16_t*)d_in[6];
  char* ws = (char*)d_ws;
  k1_qkv<<<256, 512, 0, stream>>>(x, wqkv, ws);
  k2_attn<<<256, 512, 0, stream>>>(ws);
  k3_gateup<<<224, 512, 0, stream>>>(w1, w3, ws);
  k4_final<<<256, 512, 0, stream>>>(x, wo, w2, ws, (uint16_t*)d_out);
}
```

```cpp
#include <hip/hip_runtime.h>
#include <stdint.h>

typedef __attribute__((ext_vector_type(8))) __bf16 bf16x8;
typedef __attribute__((ext_vector_type(4))) __bf16 bf16x4;
typedef __attribute__((ext_vector_type(2))) __bf16 bf16x2;
typedef __attribute__((ext_vector_type(16))) float f32x16;
typedef __attribute__((ext_vector_type(2))) float f32x2;
typedef __attribute__((ext_vector_type(4))) uint32_t u32x4;
typedef __attribute__((ext_vector_type(2))) uint32_t u32x2;
typedef __attribute__((address_space(1))) const void* gptr_t;
typedef __attribute__((address_space(3))) void* lptr_t;
typedef __attribute__((address_space(3))) bf16x4* ltr_t;
#define DEV __device__ __forceinline__

constexpr size_t WS_QF = 0;
constexpr size_t WS_KF = 2u << 20;
constexpr size_t WS_VF = 4u << 20;
constexpr size_t WS_XN = 6u << 20;
constexpr size_t WS_OF = 8u << 20;
constexpr size_t WS_HF = 10u << 20;
constexpr size_t WS_NR = 17u << 20;

#define STAMP(k, i)
DEV void glds16(const void* g, char* l) {
  __builtin_amdgcn_global_load_lds((gptr_t)g, (lptr_t)l, 16, 0, 0);
}
DEV bf16x8 lds128(const char* p) { return *(const bf16x8*)p; }
DEV bf16x8 ldstr2(const char* p, int off0, int off1) {
  bf16x4 a = __builtin_amdgcn_ds_read_tr16_b64_v4bf16((ltr_t)(p + off0));
  bf16x4 b = __builtin_amdgcn_ds_read_tr16_b64_v4bf16((ltr_t)(p + off1));
  return __builtin_shufflevector(a, b, 0, 1, 2, 3, 4, 5, 6, 7);
}
template <int OFF> DEV void rd128(u32x4& d, uint32_t a) { asm volatile("ds_read_b128 %0, %1 offset:%2" : "=v"(d) : "v"(a), "i"(OFF)); }
template <int OFF> DEV void rdtr(u32x2& d, uint32_t a) { asm volatile("ds_read_b64_tr_b16 %0, %1 offset:%2" : "=v"(d) : "v"(a), "i"(OFF)); }
struct TrF { u32x2 lo, hi; };
template <int O0, int O1> DEV void rdtr2(TrF& f, uint32_t a) { rdtr<O0>(f.lo, a); rdtr<O1>(f.hi, a); }
DEV bf16x8 asb(const TrF& f) { u32x4 r = {f.lo[0], f.lo[1], f.hi[0], f.hi[1]}; return __builtin_bit_cast(bf16x8, r); }
DEV bf16x8 asb(const u32x4& f) { return __builtin_bit_cast(bf16x8, f); }
#define LGW(n) "s_waitcnt lgkmcnt(" #n ")"
DEV uint32_t ldsaddr(const void* p) { return (uint32_t)(size_t)(__attribute__((address_space(3))) const char*)p; }
DEV f32x16 mfma(bf16x8 a, bf16x8 b, f32x16 c) { return __builtin_amdgcn_mfma_f32_32x32x16_bf16(a, b, c, 0, 0, 0); }
DEV uint32_t pk(float a, float b) { f32x2 v = {a, b}; bf16x2 r = __builtin_convertvector(v, bf16x2); return __builtin_bit_cast(uint32_t, r); }
DEV u32x4 pk8(const f32x16& a, int o) { u32x4 r = {pk(a[o], a[o + 1]), pk(a[o + 2], a[o + 3]), pk(a[o + 4], a[o + 5]), pk(a[o + 6], a[o + 7])}; return r; }
DEV float bflo(uint32_t u) { return __builtin_bit_cast(float, u << 16); }
DEV float bfhi(uint32_t u) { return __builtin_bit_cast(float, u & 0xffff0000u); }
DEV float dot2(uint32_t a, uint32_t b, float c) { return __builtin_amdgcn_fdot2_f32_bf16(__builtin_bit_cast(bf16x2, a), __builtin_bit_cast(bf16x2, b), c, false); }
template <int C> DEV float dppf(float v) { return __builtin_bit_cast(float, __builtin_amdgcn_update_dpp(0, __builtin_bit_cast(int, v), C, 0xf, 0xf, true)); }
DEV float row16_sum(float v) { v += dppf<0x128>(v); v += dppf<0x124>(v); v += dppf<0x122>(v); v += dppf<0x121>(v); return v; }
DEV float row16_max(float v) { v = fmaxf(v, dppf<0x128>(v)); v = fmaxf(v, dppf<0x124>(v)); v = fmaxf(v, dppf<0x122>(v)); v = fmaxf(v, dppf<0x121>(v)); return v; }
DEV float half_sum(float v) {
  uint32_t u = __builtin_bit_cast(uint32_t, v);
  auto r = __builtin_amdgcn_permlane32_swap(u, u, false, false);
  return __builtin_bit_cast(float, (uint32_t)r[0]) + __builtin_bit_cast(float, (uint32_t)r[1]);
}
DEV float half_max(float v) {
  uint32_t u = __builtin_bit_cast(uint32_t, v);
  auto r = __builtin_amdgcn_permlane32_swap(u, u, false, false);
  return fmaxf(__builtin_bit_cast(float, (uint32_t)r[0]), __builtin_bit_cast(float, (uint32_t)r[1]));
}
DEV float rdlane(float v, int l) { return __builtin_bit_cast(float, __builtin_amdgcn_readlane(__builtin_bit_cast(int, v), l)); }
#define WAITV(n) asm volatile("s_waitcnt vmcnt(" #n ")" ::: "memory")
#define BAR() do { asm volatile("s_waitcnt lgkmcnt(0)" ::: "memory"); __builtin_amdgcn_s_barrier(); asm volatile("" ::: "memory"); } while (0)

constexpr int K1_W = 65536, K1_ST = 24576;
template <bool QK, int KS> struct K1Step {
  static DEV void issue(TrF& wf, u32x4& x0, u32x4& x1, uint32_t wa, uint32_t xa) {
    constexpr int st = KS >> 2, kk = KS & 3;
    rdtr2<(st & 1) * K1_ST + kk * 2048, (st & 1) * K1_ST + kk * 2048 + 512>(wf, wa + ((st >> 1) & 1) * 2 * K1_ST);
    const uint32_t a = xa ^ ((KS & 7) << 5);
    rd128<(KS >> 3) * 256>(x0, a);
    if (QK) rd128<(KS >> 3) * 256 + 32768>(x1, a);
  }
};
template <bool QK, int KS, class L>
DEV void k1_steps(f32x16& a0, f32x16& a1, uint32_t wa, uint32_t xa, L& loadW, TrF& wc, u32x4& xc0, u32x4& xc1, TrF& wn, u32x4& xn0, u32x4& xn1) {
  if constexpr (KS < 31) {
    K1Step<QK, KS + 1>::issue(wn, xn0, xn1, wa, xa);
    if (QK) asm volatile(LGW(4) : "+v"(wc.lo), "+v"(wc.hi), "+v"(xc0), "+v"(xc1));
    else asm volatile(LGW(3) : "+v"(wc.lo), "+v"(wc.hi), "+v"(xc0));
  } else {
    if (QK) asm volatile(LGW(0) : "+v"(wc.lo), "+v"(wc.hi), "+v"(xc0), "+v"(xc1));
    else asm volatile(LGW(0) : "+v"(wc.lo), "+v"(wc.hi), "+v"(xc0));
  }
  if (QK) { a0 = mfma(asb(wc), asb(xc0), a0); a1 = mfma(asb(wc), asb(xc1), a1); }
  else a0 = mfma(asb(xc0), asb(wc), a0);
  __builtin_amdgcn_sched_barrier(0);
  if constexpr ((KS & 3) == 3 && KS < 28) {
    constexpr int st = KS >> 2;
    if constexpr (st < 5) WAITV(3); else WAITV(0);
    __builtin_amdgcn_s_barrier();
    if constexpr (st + 4 < 8) loadW(st + 4, st & 3);
  }
  if constexpr (KS < 31) k1_steps<QK, KS + 1>(a0, a1, wa, xa, loadW, wn, xn0, xn1, wc, xc0, xc1);
}
template <bool QK, class L> DEV void k1_loop(f32x16& a0, f32x16& a1, uint32_t wa, uint32_t xa, L& loadW) {
  TrF w0, w1; u32x4 x00, x01, x10, x11;
  K1Step<QK, 0>::issue(w0, x00, x01, wa, xa);
  k1_steps<QK, 0>(a0, a1, wa, xa, loadW, w0, x00, x01, w1, x10, x11);
}
__global__ __launch_bounds__(512) void k1_qkv(const uint16_t* __restrict__ x, const uint16_t* __restrict__ wqkv, char* __restrict__ ws) {
  __shared__ __attribute__((aligned(1024))) char sm[65536 + 4 * K1_ST];
  const int tid = threadIdx.x, lane = tid & 63, w = __builtin_amdgcn_readfirstlane(tid >> 6);
  const int b = blockIdx.x, xcd = b & 7, idx = b >> 3;
  const int tt = (xcd & 3) * 8 + (idx & 7), g = (xcd >> 2) * 4 + (idx >> 3);
  STAMP(0, 0);
#pragma unroll
  for (int i = 0; i < 8; ++i) {
    const int r = 8 * w + i;
    glds16(x + (size_t)(tt * 64 + r) * 512 + ((lane ^ (r & 15)) << 3), sm + r * 1024);
  }
  const int wrow = lane >> 3, wch = (lane & 7) ^ (((wrow >> 1) & 1) << 2);
  const uint16_t* wsrc = wqkv + (size_t)(8 * w + wrow) * 1536 + g * 64 + wch * 8;
  auto loadW = [&](int st, int buf) {
#pragma unroll
    for (int s = 0; s < 3; ++s) glds16(wsrc + (size_t)st * 64 * 1536 + s * 512, sm + K1_W + buf * K1_ST + s * 8192 + w * 1024);
  };
  loadW(0, 0); loadW(1, 1); loadW(2, 2); loadW(3, 3);
  WAITV(12);
  STAMP(0, 1);
#pragma unroll
  for (int p = 0; p < 2; ++p) {
    char* rp = sm + (8 * w + 4 * p + (lane >> 4)) * 1024 + (lane & 15) * 16;
    u32x4 v[4];
#pragma unroll
    for (int q = 0; q < 4; ++q) v[q] = *(const u32x4*)(rp + q * 256);
    float s = 0.f, ss = 0.f;
#pragma unroll
    for (int q = 0; q < 4; ++q)
#pragma unroll
      for (int e = 0; e < 4; ++e) { s = dot2(v[q][e], 0x3f803f80u, s); ss = dot2(v[q][e], v[q][e], ss); }
    s = row16_sum(s); ss = row16_sum(ss);
    const float mean = s * (1.f / 512.f);
    const float var = fmaxf(ss * (1.f / 512.f) - mean * mean, 0.f);
    const float rstd = 1.0f / sqrtf(var + 1e-5f), nb = -mean * rstd;
#pragma unroll
    for (int q = 0; q < 4; ++q) {
      u32x4 o;
#pragma unroll
      for (int e = 0; e < 4; ++e) o[e] = pk(fmaf(bflo(v[q][e]), rstd, nb), fmaf(bfhi(v[q][e]), rstd, nb));
      *(u32x4*)(rp + q * 256) = o;
    }
  }
  const int h = lane >> 5, r31 = lane & 31;
  const int xb = r31 * 1024 + ((h ^ (r31 & 15)) << 4);
  const int q4 = (lane & 15) >> 2, p4 = lane & 3, blk = (lane >> 4) & 1;
  const int ct = w < 4 ? w : 4 + (w & 1);
  const int seg = ct >> 1, c = ct & 1;
  const int wb = K1_W + seg * 8192 + (8 * h + q4) * 128 + (((4 * c + 2 * blk + (p4 >> 1)) ^ ((q4 >> 1) << 2)) << 4) + 8 * (p4 & 1);
  const int vt = (w >> 1) & 1;
  f32x16 a0 = {}, a1 = {};
  const uint32_t sb = ldsaddr(sm);
  STAMP(0, 2);
  asm volatile("s_waitcnt vmcnt(6) lgkmcnt(0)" ::: "memory");
  __builtin_amdgcn_s_barrier();
  if (w < 4) k1_loop<true>(a0, a1, sb + wb, sb + xb, loadW);
  else k1_loop<false>(a0, a1, sb + wb, sb + xb + vt * 32768, loadW);
  STAMP(0, 3);
  float* nr = (float*)(ws + WS_NR);
  if (w < 4) {
    const float sc = w < 2 ? 0.25f * 1.4426950408889634f : 1.f;
    char* dst = ws + (w < 2 ? WS_QF : WS_KF);
#pragma unroll
    for (int t = 0; t < 2; ++t) {
      f32x16 a = t ? a1 : a0;
#pragma unroll
      for (int hh = 0; hh < 2; ++hh) {
        float n2 = 0.f;
#pragma unroll
        for (int j = 0; j < 8; ++j) { a[8 * hh + j] *= sc; n2 = fmaf(a[8 * hh + j], a[8 * hh + j], n2); }
        const int head = 4 * g + 2 * c + hh, tile = 2 * tt + t;
        *(u32x4*)(dst + (size_t)(head * 64 + tile) * 1024 + lane * 16) = pk8(a, 8 * hh);
        n2 = row16_max(half_sum(n2));
        n2 = fmaxf(rdlane(n2, 0), rdlane(n2, 16));
        if (lane == 0) nr[(seg * 32 + head) * 64 + tile] = n2;
      }
    }
  } else {
    const int head = 4 * g + 2 * c + (r31 >> 4), tile = 2 * tt + vt;
    char* dst = ws + WS_VF + (size_t)(head * 64 + tile) * 1024 + h * 256 + (r31 & 15) * 16;
    *(u32x4*)(dst) = pk8(a0, 0);
    *(u32x4*)(dst + 512) = pk8(a0, 8);
    float m = 0.f;
#pragma unroll
    for (int j = 0; j < 16; ++j) m = fmaxf(m, fabsf(a0[j]));
    m = half_max(row16_max(m));
    if ((lane & 47) == 0) nr[(64 + head) * 64 + tile] = m;
  }
  {
    const int t = w >> 2, ks = 4 * g + (w & 3);
    const u32x4 v = *(const u32x4*)(sm + t * 32768 + ((xb ^ ((ks & 7) << 5)) + (ks >> 3) * 256));
    *(u32x4*)(ws + WS_XN + (size_t)((2 * tt + t) * 32 + ks) * 1024 + lane * 16) = v;
  }
  STAMP(0, 4);
}

constexpr int AT_ONES = 49152;
__global__ __launch_bounds__(512) void k2_attn(char* __restrict__ ws) {
  __shared__ __attribute__((aligned(1024))) char sm[49152 + 1024];
  const int tid = threadIdx.x, lane = tid & 63, w = __builtin_amdgcn_readfirstlane(tid >> 6);
  const int b = blockIdx.x, head = (b & 7) * 4 + (b >> 6), qg = (b >> 3) & 7;
  const char* kg = ws + WS_KF + (size_t)head * 65536 + w * 1024 + lane * 16;
  const char* vg = ws + WS_VF + (size_t)head * 65536 + w * 1024 + lane * 16;
  auto loadKV = [&](int ch, int buf) {
    glds16(kg + ch * 8192, sm + buf * 16384 + w * 1024);
    glds16(vg + ch * 8192, sm + buf * 16384 + 8192 + w * 1024);
  };
  STAMP(1, 0);
  loadKV(0, 0); loadKV(1, 1);
  const int qt = qg * 8 + w;
  const bf16x8 qf = *(const bf16x8*)(ws + WS_QF + (size_t)(head * 64 + qt) * 1024 + lane * 16);
  if (tid < 256) ((uint32_t*)(sm + AT_ONES))[tid] = 0x3f803f80u;
  const float* nr = (const float*)(ws + WS_NR);
  float kmax = nr[(32 + head) * 64 + lane], vmax = nr[(64 + head) * 64 + lane], qmax = nr[head * 64 + qt];
  kmax = row16_max(kmax); vmax = row16_max(vmax);
  kmax = fmaxf(fmaxf(rdlane(kmax, 0), rdlane(kmax, 16)), fmaxf(rdlane(kmax, 32), rdlane(kmax, 48)));
  vmax = fmaxf(fmaxf(rdlane(vmax, 0), rdlane(vmax, 16)), fmaxf(rdlane(vmax, 32), rdlane(vmax, 48)));
  const bool fast = __builtin_amdgcn_readfirstlane((int)(qmax * kmax * 1.05f <= 3600.f && vmax <= 1e15f));
  const int h = lane >> 5, m = lane & 31;
  const int vlane = h * 256 + (m & 15) * 16;
  const int vstep = m < 16 ? 1024 : 0;
  STAMP(1, 1);
  f32x16 o = {};
  float mrun = -INFINITY;
  for (int ch = 0; ch < 8; ++ch) {
    WAITV(0);
    BAR();
    if (ch < 6) loadKV(ch + 2, (ch + 2) % 3);
    const int buf = ch % 3;
    const char* kp = sm + buf * 16384 + lane * 16;
    int va = m < 16 ? buf * 16384 + 8192 + vlane : AT_ONES + vlane;
    if (fast) {
#pragma unroll
      for (int t = 0; t < 8; ++t) {
        f32x16 s = mfma(lds128(kp + t * 1024), qf, f32x16{});
        const bf16x8 v0 = lds128(sm + va), v1 = lds128(sm + va + 512);
        va += vstep;
#pragma unroll
        for (int j = 0; j < 16; ++j) s[j] = __builtin_amdgcn_exp2f(s[j]);
        o = mfma(v0, __builtin_bit_cast(bf16x8, pk8(s, 0)), o);
        o = mfma(v1, __builtin_bit_cast(bf16x8, pk8(s, 8)), o);
      }
    } else {
#pragma unroll 1
      for (int t = 0; t < 8; ++t) {
        f32x16 s = mfma(lds128(kp + t * 1024), qf, f32x16{});
        const bf16x8 v0 = lds128(sm + va), v1 = lds128(sm + va + 512);
        va += vstep;
        float mx = s[0];
#pragma unroll
        for (int j = 1; j < 16; ++j) mx = fmaxf(mx, s[j]);
        mx = fmaxf(half_max(mx), mrun);
        const float al = __builtin_amdgcn_exp2f(mrun - mx);
        mrun = mx;
#pragma unroll
        for (int j = 0; j < 16; ++j) { s[j] = __builtin_amdgcn_exp2f(s[j] - mx); o[j] *= al; }
        o = mfma(v0, __builtin_bit_cast(bf16x8, pk8(s, 0)), o);
        o = mfma(v1, __builtin_bit_cast(bf16x8, pk8(s, 8)), o);
      }
    }
  }
  STAMP(1, 2);
  const float inv = 1.0f / o[8];
#pragma unroll
  for (int j = 0; j < 8; ++j) o[j] *= inv;
  *(u32x4*)(ws + WS_OF + (size_t)(qt * 32 + head) * 1024 + lane * 16) = pk8(o, 0);
}

constexpr int GU_ST = 24576;
__global__ __launch_bounds__(512) void k3_gateup(const uint16_t* __restrict__ w1, const uint16_t* __restrict__ w3, char* __restrict__ ws) {
  __shared__ __attribute__((aligned(1024))) char sm[3 * GU_ST];
  const int tid = threadIdx.x, lane = tid & 63, w = __builtin_amdgcn_readfirstlane(tid >> 6);
  const int b = blockIdx.x, xcd = b & 7, idx = b >> 3;
  const int tg = (xcd & 3) * 4 + (idx & 3), fg = (xcd >> 2) * 7 + (idx >> 2);
  const char* xsrc = ws + WS_XN + (size_t)((tg * 4 + (w >> 1)) * 32 + (w & 1)) * 1024 + lane * 16;
  const int wrow = lane >> 4, wch = (lane & 15) ^ ((wrow & 3) << 2);
  const size_t woff = (size_t)(4 * w + wrow) * 1792 + fg * 128 + wch * 8;
  auto load = [&](int st, int buf) {
    char* d = sm + buf * GU_ST + w * 1024;
    glds16(xsrc + st * 2048, d);
    glds16(w1 + woff + (size_t)st * 32 * 1792, d + 8192);
    glds16(w3 + woff + (size_t)st * 32 * 1792, d + 16384);
  };
  STAMP(2, 0);
  load(0, 0); load(1, 1);
  const int h = lane >> 5, q4 = (lane & 15) >> 2, p4 = lane & 3, blk = (lane >> 4) & 1;
  const int c = w & 3, tp = w >> 2;
  const int wb = 8192 + (8 * h + q4) * 256 + (((4 * c + 2 * blk + (p4 >> 1)) ^ (q4 << 2)) << 4) + 8 * (p4 & 1);
  const int xb = tp * 4096 + lane * 16;
  const uint32_t sb = ldsaddr(sm);
  f32x16 g0 = {}, g1 = {}, u0 = {}, u1 = {};
#pragma unroll 1
  for (int st0 = 0; st0 < 18; st0 += 3) {
#pragma unroll
    for (int i = 0; i < 3; ++i) {
      const int st = st0 + i;
      if (st < 16) {
        if (st < 15) WAITV(3); else WAITV(0);
        BAR();
        if (st < 14) load(st + 2, (i + 2) % 3);
        const uint32_t bx = sb + xb + i * GU_ST, bw = sb + wb + i * GU_ST;
        u32x4 x0, x1, y0, y1; TrF wg, wu, vg, vu;
        rd128<0>(x0, bx); rd128<2048>(x1, bx); rdtr2<0, 1024>(wg, bw); rdtr2<8192, 8192 + 1024>(wu, bw);
        rd128<1024>(y0, bx); rd128<3072>(y1, bx); rdtr2<4096, 5120>(vg, bw); rdtr2<8192 + 4096, 8192 + 5120>(vu, bw);
        asm volatile(LGW(6) : "+v"(x0), "+v"(x1), "+v"(wg.lo), "+v"(wg.hi), "+v"(wu.lo), "+v"(wu.hi));
        g0 = mfma(asb(wg), asb(x0), g0); g1 = mfma(asb(wg), asb(x1), g1);
        u0 = mfma(asb(wu), asb(x0), u0); u1 = mfma(asb(wu), asb(x1), u1);
        __builtin_amdgcn_sched_barrier(0);
        asm volatile(LGW(0) : "+v"(y0), "+v"(y1), "+v"(vg.lo), "+v"(vg.hi), "+v"(vu.lo), "+v"(vu.hi));
        g0 = mfma(asb(vg), asb(y0), g0); g1 = mfma(asb(vg), asb(y1), g1);
        u0 = mfma(asb(vu), asb(y0), u0); u1 = mfma(asb(vu), asb(y1), u1);
        __builtin_amdgcn_sched_barrier(0);
      }
    }
  }
  STAMP(2, 1);
#pragma unroll
  for (int t = 0; t < 2; ++t) {
    f32x16 gg = t ? g1 : g0, uu = t ? u1 : u0;
#pragma unroll
    for (int j = 0; j < 16; ++j) {
      const float e = __builtin_amdgcn_exp2f(gg[j] * -1.4426950408889634f);
      gg[j] = gg[j] * __builtin_amdgcn_rcpf(1.f + e) * uu[j];
    }
    char* dst = ws + WS_HF + (size_t)((tg * 4 + tp * 2 + t) * 112 + (fg * 4 + c) * 2) * 1024 + lane * 16;
    *(u32x4*)dst = pk8(gg, 0);
    *(u32x4*)(dst + 1024) = pk8(gg, 8);
  }
}

constexpr int F_ST = 32768;
__global__ __launch_bounds__(512) void k4_final(const uint16_t* __restrict__ x, const uint16_t* __restrict__ wo, const uint16_t* __restrict__ w2,
                                                char* __restrict__ ws, uint16_t* __restrict__ out) {
  __shared__ __attribute__((aligned(1024))) char sm[3 * F_ST];
  const int tid = threadIdx.x, lane = tid & 63, w = __builtin_amdgcn_readfirstlane(tid >> 6);
  const int b = blockIdx.x, xcd = b & 7, idx = b >> 3;
  const int tg = (xcd & 3) * 8 + (idx & 7), cg = (xcd >> 2) * 4 + (idx >> 3);
  const int wrow = lane >> 3, wch = (lane & 7) ^ (((wrow >> 1) & 1) << 2);
  const size_t woff = (size_t)(16 * w + wrow) * 512 + cg * 64 + wch * 8;
  auto load = [&](int st, int buf) {
    char* d = sm + buf * F_ST;
    const char* a; const uint16_t* wp; int tstr;
    if (st < 4) { a = ws + WS_OF + (size_t)(tg * 2 * 32 + st * 8 + w) * 1024; tstr = 32 * 1024; wp = wo + (size_t)st * 128 * 512; }
    else { a = ws + WS_HF + (size_t)(tg * 2 * 112 + (st - 4) * 8 + w) * 1024; tstr = 112 * 1024; wp = w2 + (size_t)(st - 4) * 128 * 512; }
    glds16(a + lane * 16, d + w * 1024);
    glds16(a + tstr + lane * 16, d + 8192 + w * 1024);
    glds16(wp + woff, d + 16384 + w * 2048);
    glds16(wp + woff + 8 * 512, d + 16384 + w * 2048 + 1024);
  };
  STAMP(3, 0);
  load(0, 0); load(1, 1);
  const int h = lane >> 5, q4 = (lane & 15) >> 2, p4 = lane & 3, blk = (lane >> 4) & 1;
  const int c = w & 1, kq = w >> 1;
  const int wb = 16384 + kq * 4096 + (4 * h + q4) * 128 + (((4 * c + 2 * blk + (p4 >> 1)) ^ ((q4 >> 1) << 2)) << 4) + 8 * (p4 & 1);
  const int xb = kq * 2048 + lane * 16;
  const uint32_t sb = ldsaddr(sm);
  f32x16 a0 = {}, a1 = {};
#pragma unroll 1
  for (int st0 = 0; st0 < 18; st0 += 3) {
#pragma unroll
    for (int i = 0; i < 3; ++i) {
      const int st = st0 + i;
      if (st < 17) WAITV(4); else WAITV(0);
      BAR();
      if (st < 16) load(st + 2, (i + 2) % 3);
      const uint32_t bx = sb + xb + i * F_ST, bw = sb + wb + i * F_ST;
      u32x4 x0, x1, y0, y1; TrF wf, vf;
      rd128<0>(x0, bx); rd128<8192>(x1, bx); rdtr2<0, 1024>(wf, bw);
      rd128<1024>(y0, bx); rd128<8192 + 1024>(y1, bx); rdtr2<2048, 3072>(vf, bw);
      asm volatile(LGW(4) : "+v"(x0), "+v"(x1), "+v"(wf.lo), "+v"(wf.hi));
      a0 = mfma(asb(wf), asb(x0), a0); a1 = mfma(asb(wf), asb(x1), a1);
      __builtin_amdgcn_sched_barrier(0);
      asm volatile(LGW(0) : "+v"(y0), "+v"(y1), "+v"(vf.lo), "+v"(vf.hi));
      a0 = mfma(asb(vf), asb(y0), a0); a1 = mfma(asb(vf), asb(y1), a1);
      __builtin_amdgcn_sched_barrier(0);
    }
  }
  STAMP(3, 1);
  BAR();
  typedef __attribute__((ext_vector_type(4))) float f32x4;
  f32x4* red = (f32x4*)sm;
#pragma unroll
  for (int t = 0; t < 2; ++t)
#pragma unroll
    for (int i = 0; i < 4; ++i) {
      const f32x16& a = t ? a1 : a0;
      red[(((kq * 2 + c) * 2 + t) * 4 + i) * 64 + lane] = f32x4{a[4 * i], a[4 * i + 1], a[4 * i + 2], a[4 * i + 3]};
    }
  BAR();
  {
    const int c2 = w & 1, t2 = (w >> 1) & 1, ih = w >> 2;
#pragma unroll
    for (int ii = 0; ii < 2; ++ii) {
      const int i = ih * 2 + ii;
      f32x4 s = red[(((0 * 2 + c2) * 2 + t2) * 4 + i) * 64 + lane];
#pragma unroll
      for (int k = 1; k < 4; ++k) s += red[(((k * 2 + c2) * 2 + t2) * 4 + i) * 64 + lane];
      const size_t o = (size_t)(tg * 64 + t2 * 32 + (lane & 31)) * 512 + cg * 64 + c2 * 32 + 8 * i + 4 * h;
      const u32x2 xv = *(const u32x2*)(x + o);
      u32x2 r = {pk(s[0] + bflo(xv[0]), s[1] + bfhi(xv[0])), pk(s[2] + bflo(xv[1]), s[3] + bfhi(xv[1]))};
      *(u32x2*)(out + o) = r;
    }
  }
}

extern "C" void kernel_launch(void* const* d_in, const int* in_sizes, int n_in, void* d_out, int out_size, void* d_ws, size_t ws_size,
                              hipStream_t stream) {
  const uint16_t* x = (const uint16_t*)d_in[0];
  const uint16_t* wqkv = (const uint16_t*)d_in[1];
  const uint16_t* wo = (const uint16_t*)d_in[2];
  const uint16_t* w1 = (const uint16_t*)d_in[4];
  const uint16_t* w3 = (const uint16_t*)d_in[5];
  const uint16_t* w2 = (const uint16_t*)d_in[6];
  char* ws = (char*)d_ws;
  k1_qkv<<<256, 512, 0, stream>>>(x, wqkv, ws);
  k2_attn<<<256, 512, 0, stream>>>(ws);
  k3_gateup<<<224, 512, 0, stream>>>(w1, w3, ws);
  k4_final<<<256, 512, 0, stream>>>(x, wo, w2, ws, (uint16_t*)d_out);
}
```

```cpp
#include <hip/hip_runtime.h>
#include <stdint.h>

typedef __attribute__((ext_vector_type(8))) __bf16 bf16x8;
typedef __attribute__((ext_vector_type(4))) __bf16 bf16x4;
typedef __attribute__((ext_vector_type(2))) __bf16 bf16x2;
typedef __attribute__((ext_vector_type(16))) float f32x16;
typedef __attribute__((ext_vector_type(2))) float f32x2;
typedef __attribute__((ext_vector_type(4))) uint32_t u32x4;
typedef __attribute__((ext_vector_type(2))) uint32_t u32x2;
typedef __attribute__((address_space(1))) const void* gptr_t;
typedef __attribute__((address_space(3))) void* lptr_t;
typedef __attribute__((address_space(3))) bf16x4* ltr_t;
#define DEV __device__ __forceinline__
#define FAST_LIMIT 3600.f

constexpr size_t WS_QF = 0;
constexpr size_t WS_KF = 2u << 20;
constexpr size_t WS_VF = 4u << 20;
constexpr size_t WS_XN = 6u << 20;
constexpr size_t WS_OF = 8u << 20;
constexpr size_t WS_HF = 10u << 20;
constexpr size_t WS_NR = 17u << 20;
constexpr size_t WS_CT = 18u << 20;

#define STAMP(k, i)
#define STAMP_FLUSH(k)
#define STAMP_DECL
#define STAMP_ARG
#define STAMP_PASS
DEV void glds16(const void* g, char* l) {
  __builtin_amdgcn_global_load_lds((gptr_t)g, (lptr_t)l, 16, 0, 0);
}
DEV bf16x8 lds128(const char* p) { return *(const bf16x8*)p; }
DEV bf16x8 ldstr2(const char* p, int off0, int off1) {
  bf16x4 a = __builtin_amdgcn_ds_read_tr16_b64_v4bf16((ltr_t)(p + off0));
  bf16x4 b = __builtin_amdgcn_ds_read_tr16_b64_v4bf16((ltr_t)(p + off1));
  return __builtin_shufflevector(a, b, 0, 1, 2, 3, 4, 5, 6, 7);
}
template <int OFF> DEV void rd128(u32x4& d, uint32_t a) { asm volatile("ds_read_b128 %0, %1 offset:%2" : "=v"(d) : "v"(a), "i"(OFF)); }
template <int OFF> DEV void rdtr(u32x2& d, uint32_t a) { asm volatile("ds_read_b64_tr_b16 %0, %1 offset:%2" : "=v"(d) : "v"(a), "i"(OFF)); }
struct TrF { u32x2 lo, hi; };
template <int O0, int O1> DEV void rdtr2(TrF& f, uint32_t a) { rdtr<O0>(f.lo, a); rdtr<O1>(f.hi, a); }
DEV bf16x8 asb(const TrF& f) { u32x4 r = {f.lo[0], f.lo[1], f.hi[0], f.hi[1]}; return __builtin_bit_cast(bf16x8, r); }
DEV bf16x8 asb(const u32x4& f) { return __builtin_bit_cast(bf16x8, f); }
#define LGW(n) "s_waitcnt lgkmcnt(" #n ")"
DEV uint32_t ldsaddr(const void* p) { return (uint32_t)(size_t)(__attribute__((address_space(3))) const char*)p; }
DEV f32x16 mfma(bf16x8 a, bf16x8 b, f32x16 c) { return __builtin_amdgcn_mfma_f32_32x32x16_bf16(a, b, c, 0, 0, 0); }
DEV uint32_t pk(float a, float b) { f32x2 v = {a, b}; bf16x2 r = __builtin_convertvector(v, bf16x2); return __builtin_bit_cast(uint32_t, r); }
DEV u32x4 pk8(const f32x16& a, int o) { u32x4 r = {pk(a[o], a[o + 1]), pk(a[o + 2], a[o + 3]), pk(a[o + 4], a[o + 5]), pk(a[o + 6], a[o + 7])}; return r; }
DEV float bflo(uint32_t u) { return __builtin_bit_cast(float, u << 16); }
DEV float bfhi(uint32_t u) { return __builtin_bit_cast(float, u & 0xffff0000u); }
DEV float dot2(uint32_t a, uint32_t b, float c) { return __builtin_amdgcn_fdot2_f32_bf16(__builtin_bit_cast(bf16x2, a), __builtin_bit_cast(bf16x2, b), c, false); }
template <int C> DEV float dppf(float v) { return __builtin_bit_cast(float, __builtin_amdgcn_update_dpp(0, __builtin_bit_cast(int, v), C, 0xf, 0xf, true)); }
DEV float row16_sum(float v) { v += dppf<0x128>(v); v += dppf<0x124>(v); v += dppf<0x122>(v); v += dppf<0x121>(v); return v; }
DEV float row16_max(float v) { v = fmaxf(v, dppf<0x128>(v)); v = fmaxf(v, dppf<0x124>(v)); v = fmaxf(v, dppf<0x122>(v)); v = fmaxf(v, dppf<0x121>(v)); return v; }
DEV float half_sum(float v) {
  uint32_t u = __builtin_bit_cast(uint32_t, v);
  auto r = __builtin_amdgcn_permlane32_swap(u, u, false, false);
  return __builtin_bit_cast(float, (uint32_t)r[0]) + __builtin_bit_cast(float, (uint32_t)r[1]);
}
DEV float half_max(float v) {
  uint32_t u = __builtin_bit_cast(uint32_t, v);
  auto r = __builtin_amdgcn_permlane32_swap(u, u, false, false);
  return fmaxf(__builtin_bit_cast(float, (uint32_t)r[0]), __builtin_bit_cast(float, (uint32_t)r[1]));
}
DEV float rdlane(float v, int l) { return __builtin_bit_cast(float, __builtin_amdgcn_readlane(__builtin_bit_cast(int, v), l)); }
#define WAITV(n) asm volatile("s_waitcnt vmcnt(" #n ")" ::: "memory")
#define BAR() do { asm volatile("s_waitcnt lgkmcnt(0)" ::: "memory"); __builtin_amdgcn_s_barrier(); asm volatile("" ::: "memory"); } while (0)

constexpr int K1_W = 65536, K1_ST = 24576;
template <bool QK, int KS> struct K1Step {
  static DEV void issue(TrF& wf, u32x4& x0, u32x4& x1, uint32_t wa, uint32_t xa) {
    constexpr int st = KS >> 2, kk = KS & 3;
    rdtr2<(st & 1) * K1_ST + kk * 2048, (st & 1) * K1_ST + kk * 2048 + 512>(wf, wa + ((st >> 1) & 1) * 2 * K1_ST);
    const uint32_t a = xa ^ ((KS & 7) << 5);
    rd128<(KS >> 3) * 256>(x0, a);
    if (QK) rd128<(KS >> 3) * 256 + 32768>(x1, a);
  }
};
template <bool QK, int KS, class L>
DEV void k1_steps(f32x16& a0, f32x16& a1, uint32_t wa, uint32_t xa, L& loadW, TrF& wc, u32x4& xc0, u32x4& xc1, TrF& wn, u32x4& xn0, u32x4& xn1) {
  if constexpr (KS < 31) {
    K1Step<QK, KS + 1>::issue(wn, xn0, xn1, wa, xa);
    if (QK) asm volatile(LGW(4) : "+v"(wc.lo), "+v"(wc.hi), "+v"(xc0), "+v"(xc1));
    else asm volatile(LGW(3) : "+v"(wc.lo), "+v"(wc.hi), "+v"(xc0));
  } else {
    if (QK) asm volatile(LGW(0) : "+v"(wc.lo), "+v"(wc.hi), "+v"(xc0), "+v"(xc1));
    else asm volatile(LGW(0) : "+v"(wc.lo), "+v"(wc.hi), "+v"(xc0));
  }
  if (QK) { a0 = mfma(asb(wc), asb(xc0), a0); a1 = mfma(asb(wc), asb(xc1), a1); }
  else a0 = mfma(asb(xc0), asb(wc), a0);
  __builtin_amdgcn_sched_barrier(0);
  if constexpr ((KS & 3) == 3 && KS < 28) {
    constexpr int st = KS >> 2;
    if constexpr (st < 5) WAITV(3); else WAITV(0);
    __builtin_amdgcn_s_barrier();
    if constexpr (st + 4 < 8) loadW(st + 4, st & 3);
  }
  if constexpr (KS < 31) k1_steps<QK, KS + 1>(a0, a1, wa, xa, loadW, wn, xn0, xn1, wc, xc0, xc1);
}
template <bool QK, class L> DEV void k1_loop(f32x16& a0, f32x16& a1, uint32_t wa, uint32_t xa, L& loadW) {
  TrF w0, w1; u32x4 x00, x01, x10, x11;
  K1Step<QK, 0>::issue(w0, x00, x01, wa, xa);
  k1_steps<QK, 0>(a0, a1, wa, xa, loadW, w0, x00, x01, w1, x10, x11);
}
__global__ __launch_bounds__(512) void k1_qkv(const uint16_t* __restrict__ x, const uint16_t* __restrict__ wqkv, char* __restrict__ ws,
                                              const uint16_t* __restrict__ wo, const uint16_t* __restrict__ w1, const uint16_t* __restrict__ w3,
                                              const uint16_t* __restrict__ w2) {
  __shared__ __attribute__((aligned(1024))) char sm[65536 + 4 * K1_ST];
  STAMP_DECL;
  const int tid = threadIdx.x, lane = tid & 63, w = __builtin_amdgcn_readfirstlane(tid >> 6);
  const int b = blockIdx.x, xcd = b & 7, idx = b >> 3;
  const int tt = (xcd & 3) * 8 + (idx & 7), g = (xcd >> 2) * 4 + (idx >> 3);
  STAMP(0, 0);
  if (b == 0 && tid < 24) *(uint32_t*)(ws + WS_CT + tid * 256) = 0;
  uint32_t tw = 0;
#pragma unroll
  for (int i = 0; i < 8; ++i) {
    const int r = 8 * w + i;
    glds16(x + (size_t)(tt * 64 + r) * 512 + ((lane ^ (r & 15)) << 3), sm + r * 1024);
  }
  const int wrow = lane >> 3, wch = (lane & 7) ^ (((wrow >> 1) & 1) << 2);
  const uint16_t* wsrc = wqkv + (size_t)(8 * w + wrow) * 1536 + g * 64 + wch * 8;
  auto loadW = [&](int st, int buf) {
#pragma unroll
    for (int s = 0; s < 3; ++s) glds16(wsrc + (size_t)st * 64 * 1536 + s * 512, sm + K1_W + buf * K1_ST + s * 8192 + w * 1024);
  };
  loadW(0, 0); loadW(1, 1); loadW(2, 2); loadW(3, 3);
  WAITV(12);
  asm volatile("" ::"v"(tw));
  STAMP(0, 1);
#pragma unroll
  for (int p = 0; p < 2; ++p) {
    char* rp = sm + (8 * w + 4 * p + (lane >> 4)) * 1024 + (lane & 15) * 16;
    u32x4 v[4];
#pragma unroll
    for (int q = 0; q < 4; ++q) v[q] = *(const u32x4*)(rp + q * 256);
    float s = 0.f, ss = 0.f;
#pragma unroll
    for (int q = 0; q < 4; ++q)
#pragma unroll
      for (int e = 0; e < 4; ++e) { s = dot2(v[q][e], 0x3f803f80u, s); ss = dot2(v[q][e], v[q][e], ss); }
    s = row16_sum(s); ss = row16_sum(ss);
    const float mean = s * (1.f / 512.f);
    const float var = fmaxf(ss * (1.f / 512.f) - mean * mean, 0.f);
    const float rstd = 1.0f / sqrtf(var + 1e-5f), nb = -mean * rstd;
#pragma unroll
    for (int q = 0; q < 4; ++q) {
      u32x4 o;
#pragma unroll
      for (int e = 0; e < 4; ++e) o[e] = pk(fmaf(bflo(v[q][e]), rstd, nb), fmaf(bfhi(v[q][e]), rstd, nb));
      *(u32x4*)(rp + q * 256) = o;
    }
  }
  const int h = lane >> 5, r31 = lane & 31;
  const int xb = r31 * 1024 + ((h ^ (r31 & 15)) << 4);
  const int q4 = (lane & 15) >> 2, p4 = lane & 3, blk = (lane >> 4) & 1;
  const int ct = w < 4 ? w : 4 + (w & 1);
  const int seg = ct >> 1, c = ct & 1;
  const int wb = K1_W + seg * 8192 + (8 * h + q4) * 128 + (((4 * c + 2 * blk + (p4 >> 1)) ^ ((q4 >> 1) << 2)) << 4) + 8 * (p4 & 1);
  const int vt = (w >> 1) & 1;
  f32x16 a0 = {}, a1 = {};
  const uint32_t sb = ldsaddr(sm);
  STAMP(0, 2);
  asm volatile("s_waitcnt vmcnt(6) lgkmcnt(0)" ::: "memory");
  __builtin_amdgcn_s_barrier();
  if (w < 4) k1_loop<true>(a0, a1, sb + wb, sb + xb, loadW);
  else k1_loop<false>(a0, a1, sb + wb, sb + xb + vt * 32768, loadW);
  STAMP(0, 3);
  float* nr = (float*)(ws + WS_NR);
  if (w < 4) {
    const float sc = w < 2 ? 0.25f * 1.4426950408889634f : 1.f;
    char* dst = ws + (w < 2 ? WS_QF : WS_KF);
#pragma unroll
    for (int t = 0; t < 2; ++t) {
      f32x16 a = t ? a1 : a0;
#pragma unroll
      for (int hh = 0; hh < 2; ++hh) {
        float n2 = 0.f;
#pragma unroll
        for (int j = 0; j < 8; ++j) { a[8 * hh + j] *= sc; n2 = fmaf(a[8 * hh + j], a[8 * hh + j], n2); }
        const int head = 4 * g + 2 * c + hh, tile = 2 * tt + t;
        *(u32x4*)(dst + (size_t)(head * 64 + tile) * 1024 + lane * 16) = pk8(a, 8 * hh);
        n2 = row16_max(half_sum(n2));
        n2 = fmaxf(rdlane(n2, 0), rdlane(n2, 16));
        if (lane == 0) nr[(seg * 32 + head) * 64 + tile] = n2;
      }
    }
  } else {
    const int head = 4 * g + 2 * c + (r31 >> 4), tile = 2 * tt + vt;
    char* dst = ws + WS_VF + (size_t)(head * 64 + tile) * 1024 + h * 256 + (r31 & 15) * 16;
    *(u32x4*)(dst) = pk8(a0, 0);
    *(u32x4*)(dst + 512) = pk8(a0, 8);
    float m = 0.f;
#pragma unroll
    for (int j = 0; j < 16; ++j) m = fmaxf(m, fabsf(a0[j]));
    m = half_max(row16_max(m));
    if ((lane & 47) == 0) nr[(64 + head) * 64 + tile] = m;
  }
  {
    const int t = w >> 2, ks = 4 * g + (w & 3);
    const u32x4 v = *(const u32x4*)(sm + t * 32768 + ((xb ^ ((ks & 7) << 5)) + (ks >> 3) * 256));
    *(u32x4*)(ws + WS_XN + (size_t)((2 * tt + t) * 32 + ks) * 1024 + lane * 16) = v;
  }
  STAMP(0, 4);
  STAMP_FLUSH(0);
}

typedef __attribute__((address_space(1))) uint32_t* gu32_t;
DEV uint64_t sgpr64(uint64_t v) {
  return ((uint64_t)(uint32_t)__builtin_amdgcn_readfirstlane((int)(v >> 32)) << 32) | (uint32_t)__builtin_amdgcn_readfirstlane((int)v);
}
DEV void dma(uint32_t voff, uint64_t base, uint32_t lds) {
  asm volatile("s_mov_b32 m0, %2\n\ts_nop 0\n\tglobal_load_lds_dwordx4 %0, %1" ::"v"(voff), "s"(base), "s"(lds) : "memory");
}
DEV void touch(uint32_t& d, const void* p) { asm volatile("global_load_dword %0, %1, off" : "=v"(d) : "v"(p)); }
DEV void keep(uint32_t& d) { asm volatile("" ::"v"(d)); }
DEV void st_sc1(void* p, u32x4 v) { asm volatile("global_store_dwordx4 %0, %1, off sc1\n\ts_nop 1" ::"v"(p), "v"(v) : "memory"); }
DEV void signal(char* ws, int idx) {
  asm volatile("s_waitcnt vmcnt(0) lgkmcnt(0)" ::: "memory");
  __builtin_amdgcn_s_barrier();
  if (threadIdx.x == 0) __hip_atomic_fetch_add((gu32_t)(ws + WS_CT + idx * 256), 1u, __ATOMIC_RELAXED, __HIP_MEMORY_SCOPE_AGENT);
}
DEV void await(char* ws, int idx, uint32_t target) {
  if (threadIdx.x < 64) {
    for (int i = 0; i < 200000; ++i) {
      const uint32_t v = __hip_atomic_load((gu32_t)(ws + WS_CT + idx * 256), __ATOMIC_RELAXED, __HIP_MEMORY_SCOPE_AGENT);
      if (__builtin_amdgcn_readfirstlane(v) >= target) break;
      __builtin_amdgcn_s_sleep(4);
    }
  }
  asm volatile("" ::: "memory");
  __builtin_amdgcn_s_barrier();
  asm volatile("" ::: "memory");
}

constexpr int AT_ONES = 65536;
#define A_EXP(r) "v_exp_f32 v" #r ", v" #r "\n\t"
#define A_CVT(d, a, b) "v_cvt_pk_bf16_f32 v" #d ", v" #a ", v" #b "\n\t"
#define A_W "s_waitcnt lgkmcnt(2)\n\t"
#define A_RD(r, a) "ds_read_b128 " r ", " a "\n\t"
#define A_TILE(A0, A1, A2, A3, A4, A5, A6, A7, A8, A9, A10, A11, A12, A13, A14, A15, B0, B1, B15, KRD, V0RD, V1RD) \
  A_W                                                                                                         \
  "v_mfma_f32_32x32x16_bf16 v[" #B0 ":" #B15 "], v[60:63], v[56:59], 0\n\t"                                    \
  A_RD("v[60:63]", KRD)                                                                                                  \
  A_EXP(A2) A_EXP(A3) A_EXP(A4) A_EXP(A5) A_EXP(A6) A_EXP(A7)                                                 \
  A_CVT(48, A0, A1) A_CVT(49, A2, A3) A_CVT(50, A4, A5) A_EXP(A8) A_CVT(51, A6, A7) A_EXP(A9) A_EXP(A10)       \
  A_W                                                                                                         \
  "v_mfma_f32_32x32x16_bf16 v[0:15], v[64:67], v[48:51], v[0:15]\n\t"                                          \
  A_RD("v[64:67]", V0RD)                                                                                     \
  A_EXP(A11) A_EXP(A12) A_EXP(A13) A_EXP(A14) A_EXP(A15)                                                      \
  A_CVT(52, A8, A9) A_CVT(53, A10, A11) A_CVT(54, A12, A13) A_CVT(55, A14, A15)                               \
  A_EXP(B0) A_EXP(B1)                                                                                         \
  A_W                                                                                                         \
  "v_mfma_f32_32x32x16_bf16 v[0:15], v[68:71], v[52:55], v[0:15]\n\t"                                          \
  A_RD("v[68:71]", V1RD)
#define A_TILE_AB(K, V0, V1) A_TILE(16, 17, 18, 19, 20, 21, 22, 23, 24, 25, 26, 27, 28, 29, 30, 31, 32, 33, 47, K, V0, V1)
#define A_TILE_BA(K, V0, V1) A_TILE(32, 33, 34, 35, 36, 37, 38, 39, 40, 41, 42, 43, 44, 45, 46, 47, 16, 17, 31, K, V0, V1)
DEV void attn_fast(f32x16& o, u32x4 q, uint32_t kl, uint32_t vl, uint32_t vmask, uint32_t gk, uint32_t gv, uint64_t kvbase, uint32_t ldst) {
  asm volatile(
      "v_mov_b32 v72, v79\n\t"
      "v_add_u32 v73, 0x4000, v79\n\t"
      "v_mov_b32 v74, v80\n\t"
      "v_and_b32 v75, 0x4000, v76\n\t"
      "v_add_u32 v75, v75, v80\n\t"
      "s_mov_b32 s42, 0\n\t"
      "s_mov_b32 s45, 0xc000\n\t"
      "s_mov_b32 s46, 0x8000\n\t"
      "ds_read_b128 v[60:63], v72\n\t"
      "s_waitcnt lgkmcnt(0)\n\t"
      "v_mfma_f32_32x32x16_bf16 v[16:31], v[60:63], v[56:59], 0\n\t"
      "ds_read_b128 v[60:63], v72 offset:1024\n\t"
      "ds_read_b128 v[64:67], v74\n\t"
      "ds_read_b128 v[68:71], v74 offset:512\n\t"
      "s_nop 7\n\t"
      "s_nop 7\n\t"
      "v_exp_f32 v16, v16\n\t"
      "v_exp_f32 v17, v17\n\t"
      "0:\n\t"
      A_TILE_AB("v72 offset:2048", "v74 offset:1024", "v74 offset:1536")
      A_TILE_BA("v72 offset:3072", "v74 offset:2048", "v74 offset:2560")
      A_TILE_AB("v72 offset:4096", "v74 offset:3072", "v74 offset:3584")
      A_TILE_BA("v72 offset:5120", "v74 offset:4096", "v74 offset:4608")
      A_TILE_AB("v72 offset:6144", "v74 offset:5120", "v74 offset:5632")
      A_TILE_BA("v72 offset:7168", "v74 offset:6144", "v74 offset:6656")
      "s_cmp_gt_u32 s42, 4\n\t"
      "s_cbranch_scc1 1f\n\t"
      "s_waitcnt vmcnt(2)\n\t"
      "s_barrier\n\t"
      "s_add_u32 s44, s43, s45\n\t"
      "s_mov_b32 m0, s44\n\t"
      "s_add_u32 s44, s44, 0x2000\n\t"
      "global_load_lds_dwordx4 v77, s[40:41]\n\t"
      "s_mov_b32 m0, s44\n\t"
      "s_add_u32 s45, s45, 0x4000\n\t"
      "s_and_b32 s45, s45, 0xffff\n\t"
      "global_load_lds_dwordx4 v78, s[40:41]\n\t"
      "s_add_u32 s40, s40, 0x2000\n\t"
      "s_addc_u32 s41, s41, 0\n\t"
      "s_branch 2f\n\t"
      "1:\n\t"
      "s_waitcnt vmcnt(0)\n\t"
      "s_barrier\n\t"
      "2:\n\t"
      A_TILE_AB("v73", "v74 offset:7168", "v74 offset:7680")
      A_TILE_BA("v73 offset:1024", "v75", "v75 offset:512")
      "v_mov_b32 v72, v73\n\t"
      "v_mov_b32 v74, v75\n\t"
      "v_add_u32 v73, s46, v79\n\t"
      "v_and_b32 v75, s46, v76\n\t"
      "v_add_u32 v75, v75, v80\n\t"
      "s_add_u32 s46, s46, 0x4000\n\t"
      "s_and_b32 s46, s46, 0xffff\n\t"
      "s_add_u32 s42, s42, 1\n\t"
      "s_cmp_lt_u32 s42, 8\n\t"
      "s_cbranch_scc1 0b\n\t"
      "s_waitcnt lgkmcnt(0)\n\t"
      "s_nop 7\n\t"
      "s_nop 7\n\t"
      : "+{v[0:15]}"(o), "+{s[40:41]}"(kvbase)
      : "{v[56:59]}"(q), "{v79}"(kl), "{v80}"(vl), "{v76}"(vmask), "{v77}"(gk), "{v78}"(gv), "{s43}"(ldst)
      : "v16", "v17", "v18", "v19", "v20", "v21", "v22", "v23", "v24", "v25", "v26", "v27", "v28", "v29", "v30", "v31", "v32", "v33", "v34",
        "v35", "v36", "v37", "v38", "v39", "v40", "v41", "v42", "v43", "v44", "v45", "v46", "v47", "v48", "v49", "v50", "v51", "v52", "v53",
        "v54", "v55", "v60", "v61", "v62", "v63", "v64", "v65", "v66", "v67", "v68", "v69", "v70", "v71", "v72", "v73", "v74", "v75", "s42",
        "s44", "s45", "s46", "m0", "scc", "memory");
}
DEV void role_attn(char* __restrict__ ws, char* sm, int b STAMP_ARG) {
  const int tid = threadIdx.x, lane = tid & 63, w = __builtin_amdgcn_readfirstlane(tid >> 6);
  const int head = (b & 7) * 4 + (b >> 6), qg = (b >> 3) & 7;
  const char* kg = ws + WS_KF + (size_t)head * 65536 + w * 1024 + lane * 16;
  const char* vg = kg + (WS_VF - WS_KF);
  auto loadKV = [&](int ch, int buf) {
    glds16(kg + ch * 8192, sm + buf * 16384 + w * 1024);
    glds16(vg + ch * 8192, sm + buf * 16384 + 8192 + w * 1024);
  };
  STAMP(1, 0);
  const int qt = qg * 8 + w;
  const float* nr = (const float*)(ws + WS_NR);
  float kmax = nr[(32 + head) * 64 + lane], vmax = nr[(64 + head) * 64 + lane], qmax = nr[head * 64 + qg * 8 + (lane & 7)];
  const u32x4 qf = *(const u32x4*)(ws + WS_QF + (size_t)(head * 64 + qt) * 1024 + lane * 16);
  __builtin_amdgcn_sched_barrier(0);
  loadKV(0, 0); loadKV(1, 1); loadKV(2, 2);
  __builtin_amdgcn_sched_barrier(0);
  {
    const uint32_t one = (tid & 15) == 0 ? 0x3f803f80u : 0u;
    *(u32x4*)(sm + AT_ONES + tid * 16) = u32x4{one, one, one, one};
  }
  kmax = row16_max(kmax); vmax = row16_max(vmax); qmax = row16_max(qmax);
  kmax = fmaxf(fmaxf(rdlane(kmax, 0), rdlane(kmax, 16)), fmaxf(rdlane(kmax, 32), rdlane(kmax, 48)));
  vmax = fmaxf(fmaxf(rdlane(vmax, 0), rdlane(vmax, 16)), fmaxf(rdlane(vmax, 32), rdlane(vmax, 48)));
  const bool fast = __builtin_amdgcn_readfirstlane((int)(qmax * kmax * 1.05f <= FAST_LIMIT && vmax <= 1e15f));
  const int h = lane >> 5, m = lane & 31;
  const int vlane = h * 256 + (m & 15) * 16;
  STAMP(1, 1);
  f32x16 o = {};
  if (fast) {
    asm volatile("s_waitcnt vmcnt(4) lgkmcnt(0)" ::: "memory");
    __builtin_amdgcn_s_barrier();
    const uint32_t sb = ldsaddr(sm);
    attn_fast(o, qf, sb + lane * 16, sb + (m < 16 ? 8192 : AT_ONES) + vlane, m < 16 ? 0xffffffffu : 0u, w * 1024 + lane * 16,
              (uint32_t)(WS_VF - WS_KF) + w * 1024 + lane * 16, (uint64_t)(ws + WS_KF + (size_t)head * 65536 + 3 * 8192), sb + w * 1024);
  } else {
    float mrun = -INFINITY;
    const int vstep = m < 16 ? 1024 : 0;
    const bf16x8 qb = __builtin_bit_cast(bf16x8, qf);
#pragma unroll 1
    for (int ch = 0; ch < 8; ++ch) {
      WAITV(0);
      BAR();
      if (ch >= 1 && ch < 6) loadKV(ch + 2, (ch + 2) & 3);
      const int buf = ch & 3;
      const char* kp = sm + buf * 16384 + lane * 16;
      int va = m < 16 ? buf * 16384 + 8192 + vlane : AT_ONES + vlane;
#pragma unroll 1
      for (int t = 0; t < 8; ++t) {
        f32x16 s = mfma(lds128(kp + t * 1024), qb, f32x16{});
        const bf16x8 v0 = lds128(sm + va), v1 = lds128(sm + va + 512);
        va += vstep;
        float mx = s[0];
#pragma unroll
        for (int j = 1; j < 16; ++j) mx = fmaxf(mx, s[j]);
        mx = fmaxf(half_max(mx), mrun);
        const float al = __builtin_amdgcn_exp2f(mrun - mx);
        mrun = mx;
#pragma unroll
        for (int j = 0; j < 16; ++j) { s[j] = __builtin_amdgcn_exp2f(s[j] - mx); o[j] *= al; }
        o = mfma(v0, __builtin_bit_cast(bf16x8, pk8(s, 0)), o);
        o = mfma(v1, __builtin_bit_cast(bf16x8, pk8(s, 8)), o);
      }
    }
  }
  STAMP(1, 2);
  const float inv = 1.0f / half_sum(o[8]);
#pragma unroll
  for (int j = 0; j < 8; ++j) o[j] *= inv;
  st_sc1(ws + WS_OF + (size_t)(qt * 32 + head) * 1024 + lane * 16, pk8(o, 0));
  signal(ws, 16 + qg);
  STAMP(1, 3);
}

constexpr int GU_ST = 24576;
DEV void ffn_gateup(const uint16_t* __restrict__ w1, const uint16_t* __restrict__ w3, char* __restrict__ ws, char* sm, int fb STAMP_ARG) {
  const int tid = threadIdx.x, lane = tid & 63, w = __builtin_amdgcn_readfirstlane(tid >> 6);
  const int xcd = fb & 7, idx = fb >> 3;
  const int tg = (xcd & 3) * 4 + (idx & 3), fg = (xcd >> 2) * 7 + (idx >> 2);
  const uint32_t sb = __builtin_amdgcn_readfirstlane(ldsaddr(sm));
  const uint64_t xsrc = sgpr64((uint64_t)(ws + WS_XN + (size_t)((tg * 4 + (w >> 1)) * 32 + (w & 1)) * 1024));
  const int wrow = lane >> 4, wch = (lane & 15) ^ ((wrow & 3) << 2);
  const uint64_t w1s = sgpr64((uint64_t)(w1 + (size_t)(4 * w) * 1792 + fg * 128)), w3s = sgpr64((uint64_t)(w3 + (size_t)(4 * w) * 1792 + fg * 128));
  const uint32_t woff = wrow * 3584 + wch * 16, l16 = lane * 16;
  auto load = [&](int st, int buf) {
    const uint32_t d = sb + buf * GU_ST + w * 1024;
    dma(l16, xsrc + st * 2048, d);
    dma(woff, w1s + (uint64_t)st * (32 * 3584), d + 8192);
    dma(woff, w3s + (uint64_t)st * (32 * 3584), d + 16384);
  };
  uint32_t t0, t1;
  touch(t0, (const char*)(tid < 256 ? w1 : w3) + (size_t)(((tid & 255) >> 1) * 4 + (idx & 3)) * 3584 + fg * 256 + (tid & 1) * 128);
  touch(t1, ws + WS_XN + (size_t)tg * 131072 + (size_t)min((idx >> 2) * 147 + min(tid, 146), 1023) * 128);
  load(0, 0); load(1, 1);
  const int h = lane >> 5, q4 = (lane & 15) >> 2, p4 = lane & 3, blk = (lane >> 4) & 1;
  const int c = w & 3, tp = w >> 2;
  const int wb = 8192 + (8 * h + q4) * 256 + (((4 * c + 2 * blk + (p4 >> 1)) ^ (q4 << 2)) << 4) + 8 * (p4 & 1);
  const int xb = tp * 4096 + lane * 16;
  f32x16 g0 = {}, g1 = {}, u0 = {}, u1 = {};
#pragma unroll 1
  for (int st0 = 0; st0 < 18; st0 += 3) {
#pragma unroll
    for (int i = 0; i < 3; ++i) {
      const int st = st0 + i;
      if (st < 16) {
        if (st < 15) WAITV(3); else WAITV(0);
        BAR();
        if (st < 14) load(st + 2, (i + 2) % 3);
        const uint32_t bx = sb + xb + i * GU_ST, bw = sb + wb + i * GU_ST;
        u32x4 x0, x1, y0, y1; TrF wg, wu, vg, vu;
        rd128<0>(x0, bx); rd128<2048>(x1, bx); rdtr2<0, 1024>(wg, bw); rdtr2<8192, 8192 + 1024>(wu, bw);
        rd128<1024>(y0, bx); rd128<3072>(y1, bx); rdtr2<4096, 5120>(vg, bw); rdtr2<8192 + 4096, 8192 + 5120>(vu, bw);
        asm volatile(LGW(6) : "+v"(x0), "+v"(x1), "+v"(wg.lo), "+v"(wg.hi), "+v"(wu.lo), "+v"(wu.hi));
        g0 = mfma(asb(wg), asb(x0), g0); g1 = mfma(asb(wg), asb(x1), g1);
        u0 = mfma(asb(wu), asb(x0), u0); u1 = mfma(asb(wu), asb(x1), u1);
        __builtin_amdgcn_sched_barrier(0);
        asm volatile(LGW(0) : "+v"(y0), "+v"(y1), "+v"(vg.lo), "+v"(vg.hi), "+v"(vu.lo), "+v"(vu.hi));
        g0 = mfma(asb(vg), asb(y0), g0); g1 = mfma(asb(vg), asb(y1), g1);
        u0 = mfma(asb(vu), asb(y0), u0); u1 = mfma(asb(vu), asb(y1), u1);
        __builtin_amdgcn_sched_barrier(0);
      }
    }
  }
  keep(t0); keep(t1);
  STAMP(2, 1);
#pragma unroll
  for (int t = 0; t < 2; ++t) {
    f32x16 gg = t ? g1 : g0, uu = t ? u1 : u0;
#pragma unroll
    for (int j = 0; j < 16; ++j) {
      const float e = __builtin_amdgcn_exp2f(gg[j] * -1.4426950408889634f);
      gg[j] = gg[j] * __builtin_amdgcn_rcpf(1.f + e) * uu[j];
    }
    char* dst = ws + WS_HF + (size_t)((tg * 4 + tp * 2 + t) * 112 + (fg * 4 + c) * 2) * 1024 + lane * 16;
    st_sc1(dst, pk8(gg, 0));
    st_sc1(dst + 1024, pk8(gg, 8));
  }
  signal(ws, tg);
}

DEV void ld_sc1(u32x4& d, uint32_t off, uint64_t base) { asm volatile("global_load_dwordx4 %0, %1, %2 sc1" : "=v"(d) : "v"(off), "s"(base)); }
struct DnF { u32x4 a0, a1; };
template <int S> DEV void dn_mma(f32x16 (&acc)[4], DnF& f, uint32_t wa0, uint32_t wa1) {
  TrF w0, w1;
  rdtr2<S * 2048, S * 2048 + 1024>(w0, wa0); rdtr2<S * 2048, S * 2048 + 1024>(w1, wa1);
  asm volatile(LGW(2) : "+v"(w0.lo), "+v"(w0.hi));
  acc[0] = mfma(asb(w0), asb(f.a0), acc[0]); acc[1] = mfma(asb(w0), asb(f.a1), acc[1]);
  __builtin_amdgcn_sched_barrier(0);
  asm volatile(LGW(0) : "+v"(w1.lo), "+v"(w1.hi));
  acc[2] = mfma(asb(w1), asb(f.a0), acc[2]); acc[3] = mfma(asb(w1), asb(f.a1), acc[3]);
  __builtin_amdgcn_sched_barrier(0);
}
#define DN_WAIT(n, f) asm volatile("s_waitcnt vmcnt(" #n ")" : "+v"(f.a0), "+v"(f.a1)::"memory")
DEV void ffn_down(const uint16_t* __restrict__ x, const uint16_t* __restrict__ wo, const uint16_t* __restrict__ w2, char* __restrict__ ws,
                  uint16_t* __restrict__ out, char* sm, int fb STAMP_ARG) {
  const int tid = threadIdx.x, lane = tid & 63, w = __builtin_amdgcn_readfirstlane(tid >> 6);
  const int xcd = fb & 7, idx = fb >> 3;
  const int tg = (xcd & 3) * 8 + (idx & 7), cg = (xcd >> 2) * 4 + (idx >> 3);
  const int wrow = lane >> 3, wch = (lane & 7) ^ (((wrow >> 1) & 1) << 2);
  const uint32_t woff = wrow * 1024 + wch * 16;
  const uint32_t sb = __builtin_amdgcn_readfirstlane(ldsaddr(sm)), ring = sb + w * 8192;
  auto loadW = [&](uint64_t wp, uint32_t dst) {
    dma(woff, wp, dst);
    dma(woff, wp + 8 * 1024, dst + 1024);
  };
  const uint64_t w2p = sgpr64((uint64_t)(w2 + (size_t)(14 * w) * 16 * 512 + cg * 64));
  const uint64_t wop = sgpr64((uint64_t)(wo + (size_t)(4 * w) * 16 * 512 + cg * 64));
  const uint64_t hb = sgpr64((uint64_t)(ws + WS_HF + (size_t)(tg * 2 * 112 + 14 * w) * 1024));
  const uint32_t l16 = lane * 16;
  const int h = lane >> 5, q4 = (lane & 15) >> 2, p4 = lane & 3, blk = (lane >> 4) & 1;
  const uint32_t wl = (4 * h + q4) * 128 + (((2 * blk + (p4 >> 1)) ^ ((q4 >> 1) << 2)) << 4) + 8 * (p4 & 1);
  const uint32_t wa0 = ring + wl, wa1 = wa0 ^ 64;
  f32x16 acc[4] = {};
  DnF f[4];
#pragma unroll
  for (int s = 0; s < 4; ++s) loadW(w2p + s * 16384, ring + s * 2048);
  await(ws, tg >> 1, 14);
  STAMP(2, 3);
#pragma unroll
  for (int s = 0; s < 4; ++s) { ld_sc1(f[s].a0, l16 + s * 1024, hb); ld_sc1(f[s].a1, l16 + s * 1024 + 112 * 1024, hb); }
#define DN_STEP(i, n)                                                                                               \
  { constexpr int s = (i) & 3;                                                                                      \
    DN_WAIT(n, f[s]);                                                                                               \
    dn_mma<s>(acc, f[s], wa0, wa1);                                                                                 \
    if constexpr ((i) + 4 < 14) {                                                                                   \
      loadW(w2p + ((i) + 4) * 16384, ring + s * 2048);                                                                         \
      ld_sc1(f[s].a0, l16 + ((i) + 4) * 1024, hb); ld_sc1(f[s].a1, l16 + ((i) + 4) * 1024 + 112 * 1024, hb);         \
    }                                                                   \
  }
  DN_STEP(0, 6) DN_STEP(1, 8) DN_STEP(2, 10) DN_STEP(3, 12)
  DN_STEP(4, 12) DN_STEP(5, 12) DN_STEP(6, 12) DN_STEP(7, 12) DN_STEP(8, 12) DN_STEP(9, 12)
  DN_STEP(10, 12) DN_STEP(11, 8) DN_STEP(12, 4) DN_STEP(13, 0)
#undef DN_STEP
  STAMP(2, 4);
  typedef __attribute__((ext_vector_type(4))) float f32x4;
  f32x4* red = (f32x4*)sm;
  const int row = 4 * w + (lane >> 4), cq = lane & 15;
  const size_t oo = (size_t)(tg * 64 + row) * 512 + cg * 64 + (cq >> 1) * 8 + (cq & 1) * 4;
  const u32x2 xv0 = *(const u32x2*)(x + oo), xv1 = *(const u32x2*)(x + oo + 32 * 512);
  const int rpos = (row + 32 * (cq & 1)) ^ ((cq >> 1) << 1) ^ (cq & 1);
  f32x4 part[2];
#pragma unroll
  for (int t = 0; t < 2; ++t) {
    BAR();
#pragma unroll
    for (int c = 0; c < 2; ++c)
#pragma unroll
      for (int i = 0; i < 4; ++i) {
        const f32x16& a = acc[c * 2 + t];
        red[(w * 8 + c * 4 + i) * 64 + (lane ^ ((c * 4 + i) << 1) ^ h)] = f32x4{a[4 * i], a[4 * i + 1], a[4 * i + 2], a[4 * i + 3]};
      }
    BAR();
    f32x4 s = red[(cq >> 1) * 64 + rpos];
#pragma unroll
    for (int k = 1; k < 8; ++k) s += red[(k * 8 + (cq >> 1)) * 64 + rpos];
    const u32x2 xv = t ? xv1 : xv0;
    part[t] = s + f32x4{bflo(xv[0]), bfhi(xv[0]), bflo(xv[1]), bfhi(xv[1])};
  }
  BAR();
#pragma unroll
  for (int s = 0; s < 4; ++s) loadW(wop + s * 16384, sb + (4 * w + s) * 2048);
  const int t2 = w & 1, c2 = (w >> 1) & 1, kh = w >> 2;
  const uint64_t ob = sgpr64((uint64_t)(ws + WS_OF + (size_t)((tg * 2 + t2) * 32 + 16 * kh) * 1024));
  WAITV(0);
  STAMP(2, 5);
  await(ws, 16 + (tg >> 2), 32);
  STAMP(2, 6);
  {
    u32x4 f[16];
#pragma unroll
    for (int s = 0; s < 16; ++s) ld_sc1(f[s], l16 + s * 1024, ob);
    const uint32_t wa = sb + kh * 32768 + (wl ^ (c2 * 64));
    f32x16 a0 = {}, a1 = {};
    TrF w0, w1;
#define TL_STEP(s, n)                                                                     \
    { rdtr2<(s) * 2048, (s) * 2048 + 1024>(w0, wa);                                       \
      rdtr2<(s) * 2048 + 2048, (s) * 2048 + 3072>(w1, wa);                                \
      asm volatile("s_waitcnt vmcnt(" #n ") lgkmcnt(0)" : "+v"(f[s]), "+v"(f[(s) + 1]), "+v"(w0.lo), "+v"(w0.hi), "+v"(w1.lo), "+v"(w1.hi)); \
      a0 = mfma(asb(w0), asb(f[s]), a0); a1 = mfma(asb(w1), asb(f[(s) + 1]), a1);          \
      __builtin_amdgcn_sched_barrier(0); }
    TL_STEP(0, 14) TL_STEP(2, 12) TL_STEP(4, 10) TL_STEP(6, 8) TL_STEP(8, 6) TL_STEP(10, 4) TL_STEP(12, 2) TL_STEP(14, 0)
#undef TL_STEP
    a0 += a1;

    BAR();
#pragma unroll
    for (int i = 0; i < 4; ++i)
      red[((kh * 2 + t2) * 8 + c2 * 4 + i) * 64 + (lane ^ ((c2 * 4 + i) << 1) ^ h)] = f32x4{a0[4 * i], a0[4 * i + 1], a0[4 * i + 2], a0[4 * i + 3]};
    BAR();
#pragma unroll
    for (int t = 0; t < 2; ++t) {
      const f32x4 s = part[t] + red[(t * 8 + (cq >> 1)) * 64 + rpos] + red[((2 + t) * 8 + (cq >> 1)) * 64 + rpos];
      *(u32x2*)(out + oo + t * 32 * 512) = u32x2{pk(s[0], s[1]), pk(s[2], s[3])};
    }
  }
  STAMP(2, 7);
}

__global__ __launch_bounds__(512, 4) void k2_fused(const uint16_t* __restrict__ x, const uint16_t* __restrict__ wo, const uint16_t* __restrict__ w1,
                                                   const uint16_t* __restrict__ w3, const uint16_t* __restrict__ w2, char* __restrict__ ws,
                                                   uint16_t* __restrict__ out) {
  __shared__ __attribute__((aligned(1024))) char sm[81920];
  const int b = blockIdx.x;
  STAMP_DECL;
  if (b < 256) { role_attn(ws, sm, b STAMP_PASS); STAMP_FLUSH(1); return; }
  const int fb = b - 256;
#define PRIO 3
  __builtin_amdgcn_s_setprio(PRIO);
  STAMP(2, 0);
  uint32_t tw;
  {
    const int xcd = fb & 7, idx = fb >> 3, cg = (xcd >> 2) * 4 + (idx >> 3), r = min((int)threadIdx.x, 287) * 8 + (idx & 7);
    touch(tw, (const char*)(r < 1792 ? w2 + (size_t)r * 512 : wo + (size_t)(r - 1792) * 512) + cg * 128);
  }
  if (fb < 224) ffn_gateup(w1, w3, ws, sm, fb STAMP_PASS);
  else WAITV(0);
  keep(tw);
  STAMP(2, 2);
  ffn_down(x, wo, w2, ws, out, sm, fb STAMP_PASS);
  STAMP_FLUSH(2);
}

extern "C" void kernel_launch(void* const* d_in, const int* in_sizes, int n_in, void* d_out, int out_size, void* d_ws, size_t ws_size,
                              hipStream_t stream) {
  const uint16_t* x = (const uint16_t*)d_in[0];
  const uint16_t* wqkv = (const uint16_t*)d_in[1];
  const uint16_t* wo = (const uint16_t*)d_in[2];
  const uint16_t* w1 = (const uint16_t*)d_in[4];
  const uint16_t* w3 = (const uint16_t*)d_in[5];
  const uint16_t* w2 = (const uint16_t*)d_in[6];
  char* ws = (char*)d_ws;
  k1_qkv<<<256, 512, 0, stream>>>(x, wqkv, ws, wo, w1, w3, w2);
  k2_fused<<<512, 512, 0, stream>>>(x, wo, w1, w3, w2, ws, (uint16_t*)d_out);
}
```

```cpp
#include <hip/hip_runtime.h>
#include <stdint.h>

typedef __attribute__((ext_vector_type(8))) __bf16 bf16x8;
typedef __attribute__((ext_vector_type(4))) __bf16 bf16x4;
typedef __attribute__((ext_vector_type(2))) __bf16 bf16x2;
typedef __attribute__((ext_vector_type(16))) float f32x16;
typedef __attribute__((ext_vector_type(2))) float f32x2;
typedef __attribute__((ext_vector_type(4))) uint32_t u32x4;
typedef __attribute__((ext_vector_type(2))) uint32_t u32x2;
typedef __attribute__((address_space(1))) const void* gptr_t;
typedef __attribute__((address_space(3))) void* lptr_t;
typedef __attribute__((address_space(3))) bf16x4* ltr_t;
#define FAST_LIMIT 3600.f
#define APRIO 3
#define ALO 1
#define PRIO 2
#define PRIO2 0
#define FDELAY 40
#define PFB 0
#define PFS 24
#define PSLEEP 2
#define K1ST " sc1"
#define OUTST " sc1"
#define DEV __device__ __forceinline__

constexpr size_t WS_QF = 0;
constexpr size_t WS_KF = 2u << 20;
constexpr size_t WS_VF = 4u << 20;
constexpr size_t WS_XN = 6u << 20;
constexpr size_t WS_OF = 8u << 20;
constexpr size_t WS_HF = 10u << 20;
constexpr size_t WS_NR = 17u << 20;
constexpr size_t WS_CT = 18u << 20;
DEV void glds16(const void* g, char* l) {
  __builtin_amdgcn_global_load_lds((gptr_t)g, (lptr_t)l, 16, 0, 0);
}
DEV bf16x8 lds128(const char* p) { return *(const bf16x8*)p; }
DEV bf16x8 ldstr2(const char* p, int off0, int off1) {
  bf16x4 a = __builtin_amdgcn_ds_read_tr16_b64_v4bf16((ltr_t)(p + off0));
  bf16x4 b = __builtin_amdgcn_ds_read_tr16_b64_v4bf16((ltr_t)(p + off1));
  return __builtin_shufflevector(a, b, 0, 1, 2, 3, 4, 5, 6, 7);
}
template <int OFF> DEV void rd128(u32x4& d, uint32_t a) { asm volatile("ds_read_b128 %0, %1 offset:%2" : "=v"(d) : "v"(a), "i"(OFF)); }
template <int OFF> DEV void rdtr(u32x2& d, uint32_t a) { asm volatile("ds_read_b64_tr_b16 %0, %1 offset:%2" : "=v"(d) : "v"(a), "i"(OFF)); }
struct TrF { u32x2 lo, hi; };
template <int O0, int O1> DEV void rdtr2(TrF& f, uint32_t a) { rdtr<O0>(f.lo, a); rdtr<O1>(f.hi, a); }
DEV bf16x8 asb(const TrF& f) { u32x4 r = {f.lo[0], f.lo[1], f.hi[0], f.hi[1]}; return __builtin_bit_cast(bf16x8, r); }
DEV bf16x8 asb(const u32x4& f) { return __builtin_bit_cast(bf16x8, f); }
#define LGW(n) "s_waitcnt lgkmcnt(" #n ")"
DEV uint32_t ldsaddr(const void* p) { return (uint32_t)(size_t)(__attribute__((address_space(3))) const char*)p; }
DEV f32x16 mfma(bf16x8 a, bf16x8 b, f32x16 c) { return __builtin_amdgcn_mfma_f32_32x32x16_bf16(a, b, c, 0, 0, 0); }
DEV uint32_t pk(float a, float b) { f32x2 v = {a, b}; bf16x2 r = __builtin_convertvector(v, bf16x2); return __builtin_bit_cast(uint32_t, r); }
DEV u32x4 pk8(const f32x16& a, int o) { u32x4 r = {pk(a[o], a[o + 1]), pk(a[o + 2], a[o + 3]), pk(a[o + 4], a[o + 5]), pk(a[o + 6], a[o + 7])}; return r; }
DEV float bflo(uint32_t u) { return __builtin_bit_cast(float, u << 16); }
DEV float bfhi(uint32_t u) { return __builtin_bit_cast(float, u & 0xffff0000u); }
DEV float dot2(uint32_t a, uint32_t b, float c) { return __builtin_amdgcn_fdot2_f32_bf16(__builtin_bit_cast(bf16x2, a), __builtin_bit_cast(bf16x2, b), c, false); }
template <int C> DEV float dppf(float v) { return __builtin_bit_cast(float, __builtin_amdgcn_update_dpp(0, __builtin_bit_cast(int, v), C, 0xf, 0xf, true)); }
DEV float row16_sum(float v) { v += dppf<0x128>(v); v += dppf<0x124>(v); v += dppf<0x122>(v); v += dppf<0x121>(v); return v; }
DEV float row16_max(float v) { v = fmaxf(v, dppf<0x128>(v)); v = fmaxf(v, dppf<0x124>(v)); v = fmaxf(v, dppf<0x122>(v)); v = fmaxf(v, dppf<0x121>(v)); return v; }
DEV float half_sum(float v) {
  uint32_t u = __builtin_bit_cast(uint32_t, v);
  auto r = __builtin_amdgcn_permlane32_swap(u, u, false, false);
  return __builtin_bit_cast(float, (uint32_t)r[0]) + __builtin_bit_cast(float, (uint32_t)r[1]);
}
DEV float half_max(float v) {
  uint32_t u = __builtin_bit_cast(uint32_t, v);
  auto r = __builtin_amdgcn_permlane32_swap(u, u, false, false);
  return fmaxf(__builtin_bit_cast(float, (uint32_t)r[0]), __builtin_bit_cast(float, (uint32_t)r[1]));
}
DEV float rdlane(float v, int l) { return __builtin_bit_cast(float, __builtin_amdgcn_readlane(__builtin_bit_cast(int, v), l)); }
DEV void st_wt(void* p, u32x4 v) { asm volatile("global_store_dwordx4 %0, %1, off" K1ST "\n\ts_nop 1" ::"v"(p), "v"(v) : "memory"); }
DEV void st_wt2(void* p, u32x2 v) { asm volatile("global_store_dwordx2 %0, %1, off" OUTST "\n\ts_nop 1" ::"v"(p), "v"(v) : "memory"); }
#define WAITV(n) asm volatile("s_waitcnt vmcnt(" #n ")" ::: "memory")
#define BAR() do { asm volatile("s_waitcnt lgkmcnt(0)" ::: "memory"); __builtin_amdgcn_s_barrier(); asm volatile("" ::: "memory"); } while (0)

#define CODE_ADDR(dst, sym) asm volatile("s_getpc_b64 s[60:61]\n\ts_add_u32 s60, s60, " sym "@rel32@lo+4\n\ts_addc_u32 s61, s61, " sym "@rel32@hi+12\n\ts_mov_b64 %0, s[60:61]" : "=s"(dst)::"s60", "s61", "scc")
#define K1_SYM "_Z6k1_qkvPKtS0_PcS0_S0_S0_S0_"
#define K2_SYM "_Z8k2_fusedPKtS0_S0_S0_S0_PcPt"
constexpr int K1_LINES = 76, K2_LINES = 100;
constexpr int K1_W = 65536, K1_ST = 24576;
template <bool QK, int KS> struct K1Step {
  static DEV void issue(TrF& wf, u32x4& x0, u32x4& x1, uint32_t wa, uint32_t xa) {
    constexpr int st = KS >> 2, kk = KS & 3;
    rdtr2<(st & 1) * K1_ST + kk * 2048, (st & 1) * K1_ST + kk * 2048 + 512>(wf, wa + ((st >> 1) & 1) * 2 * K1_ST);
    const uint32_t a = xa ^ ((KS & 7) << 5);
    rd128<(KS >> 3) * 256>(x0, a);
    if (QK) rd128<(KS >> 3) * 256 + 32768>(x1, a);
  }
};
struct K1F { TrF w; u32x4 x0, x1; };
template <bool QK, int KS, class L>
DEV void k1_steps(f32x16& a0, f32x16& a1, uint32_t wa, uint32_t xa, L& loadW, K1F& c, K1F& n, K1F& m) {
  if constexpr (KS < 30) K1Step<QK, KS + 2>::issue(m.w, m.x0, m.x1, wa, xa);
  constexpr int later = KS < 30 ? 2 : 31 - KS;
  if constexpr (QK) asm volatile("s_waitcnt lgkmcnt(%4)" : "+v"(c.w.lo), "+v"(c.w.hi), "+v"(c.x0), "+v"(c.x1) : "i"(4 * later));
  else asm volatile("s_waitcnt lgkmcnt(%3)" : "+v"(c.w.lo), "+v"(c.w.hi), "+v"(c.x0) : "i"(3 * later));
  if (QK) { a0 = mfma(asb(c.w), asb(c.x0), a0); a1 = mfma(asb(c.w), asb(c.x1), a1); }
  else a0 = mfma(asb(c.x0), asb(c.w), a0);
  __builtin_amdgcn_sched_barrier(0);
  if constexpr ((KS & 3) == 3 && KS < 24) {
    constexpr int st = KS >> 2;
    if constexpr (st < 5) WAITV(3); else WAITV(0);
    __builtin_amdgcn_s_barrier();
    if constexpr (st + 4 < 8) loadW(st + 4, st & 3);
  }
  if constexpr (KS < 31) k1_steps<QK, KS + 1>(a0, a1, wa, xa, loadW, n, m, c);
}
template <bool QK, class L> DEV void k1_loop(f32x16& a0, f32x16& a1, uint32_t wa, uint32_t xa, L& loadW) {
  K1F f0, f1, f2;
  K1Step<QK, 0>::issue(f0.w, f0.x0, f0.x1, wa, xa);
  K1Step<QK, 1>::issue(f1.w, f1.x0, f1.x1, wa, xa);
  k1_steps<QK, 0>(a0, a1, wa, xa, loadW, f0, f1, f2);
}
__global__ __launch_bounds__(576) void k1_qkv(const uint16_t* __restrict__ x, const uint16_t* __restrict__ wqkv, char* __restrict__ ws,
                                              const uint16_t* __restrict__ wo, const uint16_t* __restrict__ w1, const uint16_t* __restrict__ w3,
                                              const uint16_t* __restrict__ w2) {
  __shared__ __attribute__((aligned(1024))) char sm[65536 + 4 * K1_ST];
  const int tid = threadIdx.x, lane = tid & 63, w = __builtin_amdgcn_readfirstlane(tid >> 6);
  const int b = blockIdx.x, xcd = b & 7, idx = b >> 3;
  const int tt = (xcd & 3) * 8 + (idx & 7), g = (xcd >> 2) * 4 + (idx >> 3);
  if (b == 0 && tid < 80) *(uint32_t*)(ws + WS_CT + tid * 256) = 0;
  if (w == 8) {
    uint32_t u0, u1;
    uint32_t c0, c1, c2, c3;
    {
      uint64_t k1c, k2c;
      CODE_ADDR(k1c, K1_SYM); CODE_ADDR(k2c, K2_SYM);
      const uint32_t l = lane * 128;
      asm volatile("global_load_dword %0, %1, %2" : "=v"(c0) : "v"(l), "s"(k1c));
      asm volatile("global_load_dword %0, %1, %2" : "=v"(c1) : "v"(min(l + 8192u, (K1_LINES - 1) * 128u)), "s"(k1c));
      asm volatile("global_load_dword %0, %1, %2" : "=v"(c2) : "v"(l), "s"(k2c));
      asm volatile("global_load_dword %0, %1, %2" : "=v"(c3) : "v"(min(l + 8192u, (K2_LINES - 1) * 128u)), "s"(k2c));
    }
    __builtin_amdgcn_s_sleep(PFS);
    {
      const int l0 = (idx & 7) * 96 + lane, l1 = (idx & 7) * 96 + 64 + (lane & 31);
      asm volatile("global_load_dword %0, %1, off" : "=v"(u0) : "v"(wqkv + (size_t)(256 + l0 / 3) * 1536 + (l0 % 3) * 512 + g * 64));
      asm volatile("global_load_dword %0, %1, off" : "=v"(u1) : "v"(wqkv + (size_t)(256 + l1 / 3) * 1536 + (l1 % 3) * 512 + g * 64));
    }
#pragma unroll
    for (int i = 0; i < PFB; ++i) __builtin_amdgcn_s_barrier();
    uint32_t t0, t1, t2;
    const size_t o = (size_t)(b * 56 + min(lane, 55)) * 128;
    asm volatile("global_load_dword %0, %1, off" : "=v"(t0) : "v"((const char*)w1 + o));
    asm volatile("global_load_dword %0, %1, off" : "=v"(t1) : "v"((const char*)w3 + o));
    const char* p = lane < 48 ? (const char*)w2 + (size_t)(b * 56 + lane) * 128 : (const char*)wo + (size_t)(b * 16 + lane - 48) * 128;
    asm volatile("global_load_dword %0, %1, off" : "=v"(t2) : "v"(p));
    uint32_t t3;
    asm volatile("global_load_dword %0, %1, off" : "=v"(t3) : "v"((const char*)w2 + (size_t)(b * 56 + 48 + (lane & 7)) * 128));
#pragma unroll
    for (int i = PFB; i < 7; ++i) __builtin_amdgcn_s_barrier();
    asm volatile("s_waitcnt vmcnt(0)" ::"v"(t0), "v"(t1), "v"(t2), "v"(t3) : "memory");
    asm volatile("s_waitcnt vmcnt(0)" ::"v"(u0), "v"(u1) : "memory");
    asm volatile("" ::"v"(c0), "v"(c1), "v"(c2), "v"(c3));
    return;
  }
#pragma unroll
  for (int i = 0; i < 8; ++i) {
    const int r = 8 * w + i;
    glds16(x + (size_t)(tt * 64 + r) * 512 + ((lane ^ (r & 15)) << 3), sm + r * 1024);
  }
  const int wrow = lane >> 3, wch = (lane & 7) ^ (((wrow >> 1) & 1) << 2);
  const uint16_t* wsrc = wqkv + (size_t)(8 * w + wrow) * 1536 + g * 64 + wch * 8;
  auto loadW = [&](int st, int buf) {
#pragma unroll
    for (int s = 0; s < 3; ++s) glds16(wsrc + (size_t)st * 64 * 1536 + s * 512, sm + K1_W + buf * K1_ST + s * 8192 + w * 1024);
  };
  loadW(0, 0); loadW(1, 1);
  WAITV(6);
  loadW(2, 2); loadW(3, 3);
#pragma unroll
  for (int p = 0; p < 2; ++p) {
    char* rp = sm + (8 * w + 4 * p + (lane >> 4)) * 1024 + (lane & 15) * 16;
    u32x4 v[4];
#pragma unroll
    for (int q = 0; q < 4; ++q) v[q] = *(const u32x4*)(rp + q * 256);
    float s = 0.f, ss = 0.f;
#pragma unroll
    for (int q = 0; q < 4; ++q)
#pragma unroll
      for (int e = 0; e < 4; ++e) { s = dot2(v[q][e], 0x3f803f80u, s); ss = dot2(v[q][e], v[q][e], ss); }
    s = row16_sum(s); ss = row16_sum(ss);
    const float mean = s * (1.f / 512.f);
    const float var = fmaxf(ss * (1.f / 512.f) - mean * mean, 0.f);
    const float rstd = 1.0f / sqrtf(var + 1e-5f), nb = -mean * rstd;
#pragma unroll
    for (int q = 0; q < 4; ++q) {
      u32x4 o;
#pragma unroll
      for (int e = 0; e < 4; ++e) o[e] = pk(fmaf(bflo(v[q][e]), rstd, nb), fmaf(bfhi(v[q][e]), rstd, nb));
      *(u32x4*)(rp + q * 256) = o;
    }
  }
  const int h = lane >> 5, r31 = lane & 31;
  const int xb = r31 * 1024 + ((h ^ (r31 & 15)) << 4);
  const int q4 = (lane & 15) >> 2, p4 = lane & 3, blk = (lane >> 4) & 1;
  const int ct = w < 4 ? w : 4 + (w & 1);
  const int seg = ct >> 1, c = ct & 1;
  const int wb = K1_W + seg * 8192 + (8 * h + q4) * 128 + (((4 * c + 2 * blk + (p4 >> 1)) ^ ((q4 >> 1) << 2)) << 4) + 8 * (p4 & 1);
  const int vt = (w >> 1) & 1;
  f32x16 a0 = {}, a1 = {};
  const uint32_t sb = ldsaddr(sm);
  asm volatile("s_waitcnt vmcnt(6) lgkmcnt(0)" ::: "memory");
  __builtin_amdgcn_s_barrier();
  if (w < 4) k1_loop<true>(a0, a1, sb + wb, sb + xb, loadW);
  else k1_loop<false>(a0, a1, sb + wb, sb + xb + vt * 32768, loadW);
  float* nr = (float*)(ws + WS_NR);
  if (w < 4) {
    const float sc = w < 2 ? 0.25f * 1.4426950408889634f : 1.f;
    char* dst = ws + (w < 2 ? WS_QF : WS_KF);
#pragma unroll
    for (int t = 0; t < 2; ++t) {
      f32x16 a = t ? a1 : a0;
#pragma unroll
      for (int hh = 0; hh < 2; ++hh) {
        float n2 = 0.f;
#pragma unroll
        for (int j = 0; j < 8; ++j) { a[8 * hh + j] *= sc; n2 = fmaf(a[8 * hh + j], a[8 * hh + j], n2); }
        const int head = 4 * g + 2 * c + hh, tile = 2 * tt + t;
        st_wt(dst + (size_t)(head * 64 + tile) * 1024 + lane * 16, pk8(a, 8 * hh));
        n2 = row16_max(half_sum(n2));
        n2 = fmaxf(rdlane(n2, 0), rdlane(n2, 16));
        if (lane == 0) nr[(seg * 32 + head) * 64 + tile] = n2;
      }
    }
  } else {
    const int head = 4 * g + 2 * c + (r31 >> 4), tile = 2 * tt + vt;
    char* dst = ws + WS_VF + (size_t)(head * 64 + tile) * 1024 + h * 256 + (r31 & 15) * 16;
    st_wt(dst, pk8(a0, 0));
    st_wt(dst + 512, pk8(a0, 8));
    float m = 0.f;
#pragma unroll
    for (int j = 0; j < 16; ++j) m = fmaxf(m, fabsf(a0[j]));
    m = half_max(row16_max(m));
    if ((lane & 47) == 0) nr[(64 + head) * 64 + tile] = m;
  }
  {
    const int t = w >> 2, ks = 4 * g + (w & 3);
    const u32x4 v = *(const u32x4*)(sm + t * 32768 + ((xb ^ ((ks & 7) << 5)) + (ks >> 3) * 256));
    st_wt(ws + WS_XN + (size_t)((2 * tt + t) * 32 + ks) * 1024 + lane * 16, v);
  }
}

typedef __attribute__((address_space(1))) uint32_t* gu32_t;
DEV uint64_t sgpr64(uint64_t v) {
  return ((uint64_t)(uint32_t)__builtin_amdgcn_readfirstlane((int)(v >> 32)) << 32) | (uint32_t)__builtin_amdgcn_readfirstlane((int)v);
}
DEV void dma(uint32_t voff, uint64_t base, uint32_t lds) {
  asm volatile("s_mov_b32 m0, %2\n\ts_nop 0\n\tglobal_load_lds_dwordx4 %0, %1" ::"v"(voff), "s"(base), "s"(lds) : "memory");
}
DEV void st_sc1(void* p, u32x4 v) { asm volatile("global_store_dwordx4 %0, %1, off sc1\n\ts_nop 1" ::"v"(p), "v"(v) : "memory"); }
DEV void signal(char* ws, int idx) {
  asm volatile("s_waitcnt vmcnt(0) lgkmcnt(0)" ::: "memory");
  __builtin_amdgcn_s_barrier();
  if (threadIdx.x == 0) __hip_atomic_fetch_add((gu32_t)(ws + WS_CT + idx * 256), 1u, __ATOMIC_RELAXED, __HIP_MEMORY_SCOPE_AGENT);
}
DEV void await(char* ws, int idx, uint32_t target) {
  if (threadIdx.x < 64) {
    for (int i = 0; i < 200000; ++i) {
      const uint32_t v = __hip_atomic_load((gu32_t)(ws + WS_CT + idx * 256), __ATOMIC_RELAXED, __HIP_MEMORY_SCOPE_AGENT);
      if (__builtin_amdgcn_readfirstlane(v) >= target) break;
      __builtin_amdgcn_s_sleep(4);
    }
  }
  asm volatile("" ::: "memory");
  __builtin_amdgcn_s_barrier();
  asm volatile("" ::: "memory");
}

#define P_STR2(x) #x
#define P_STR(x) P_STR2(x)
DEV void await_wave(uint64_t ctr, uint32_t target) {
  uint32_t n = 200000, t;
  asm volatile("1:\n\tglobal_load_dword %0, %2, %3 sc1\n\ts_waitcnt vmcnt(0)\n\tv_cmp_le_u32 vcc, %4, %0\n\ts_cbranch_vccnz 2f\n\t"
               "s_sleep " P_STR(PSLEEP) "\n\ts_sub_u32 %1, %1, 1\n\ts_cmp_lg_u32 %1, 0\n\ts_cbranch_scc1 1b\n2:"
               : "=&v"(t), "+s"(n) : "v"(0u), "s"(ctr), "s"(target) : "vcc", "scc", "memory");
}

constexpr int AT_ONES = 65536;
#define A_EXP(r) "v_exp_f32 v" #r ", v" #r "\n\t"
#define A_CVT(d, a, b) "v_cvt_pk_bf16_f32 v" #d ", v" #a ", v" #b "\n\t"
#define A_W "s_waitcnt lgkmcnt(2)\n\t"
#define A_STR2(x) #x
#define A_STR(x) A_STR2(x)
#define A_HI "s_setprio " A_STR(APRIO) "\n\t"
#define A_LO "s_setprio " A_STR(ALO) "\n\t"
#define A_RD(r, a) "ds_read_b128 " r ", " a "\n\t"
#define A_TILE(A0, A1, A2, A3, A4, A5, A6, A7, A8, A9, A10, A11, A12, A13, A14, A15, B0, B1, B15, KRD, V0RD, V1RD) \
  A_W                                                                                                         \
  "v_mfma_f32_32x32x16_bf16 v[" #B0 ":" #B15 "], v[60:63], v[56:59], 0\n\t"                                    \
  A_LO                                                                                                         \
  A_RD("v[60:63]", KRD)                                                                                                  \
  A_EXP(A2) A_EXP(A3) A_EXP(A4) A_EXP(A5) A_EXP(A6) A_EXP(A7)                                                 \
  A_CVT(48, A0, A1) A_CVT(49, A2, A3) A_CVT(50, A4, A5) A_EXP(A8) A_CVT(51, A6, A7) A_EXP(A9) A_EXP(A10)       \
  A_W A_HI                                                                                                    \
  "v_mfma_f32_32x32x16_bf16 v[0:15], v[64:67], v[48:51], v[0:15]\n\t"                                          \
  A_LO                                                                                                         \
  A_RD("v[64:67]", V0RD)                                                                                     \
  A_EXP(A11) A_EXP(A12) A_EXP(A13) A_EXP(A14) A_EXP(A15)                                                      \
  A_CVT(52, A8, A9) A_CVT(53, A10, A11) A_CVT(54, A12, A13) A_CVT(55, A14, A15)                               \
  A_EXP(B0) A_EXP(B1)                                                                                         \
  A_W A_HI                                                                                                    \
  "v_mfma_f32_32x32x16_bf16 v[0:15], v[68:71], v[52:55], v[0:15]\n\t"                                          \
  A_RD("v[68:71]", V1RD)
#define A_TILE_AB(K, V0, V1) A_TILE(16, 17, 18, 19, 20, 21, 22, 23, 24, 25, 26, 27, 28, 29, 30, 31, 32, 33, 47, K, V0, V1)
#define A_TILE_BA(K, V0, V1) A_TILE(32, 33, 34, 35, 36, 37, 38, 39, 40, 41, 42, 43, 44, 45, 46, 47, 16, 17, 31, K, V0, V1)
DEV void attn_fast(f32x16& o, u32x4 q, uint32_t kl, uint32_t vl, uint32_t vmask, uint32_t gk, uint32_t gv, uint64_t kvbase, uint32_t ldst) {
  asm volatile(
      "v_mov_b32 v72, v79\n\t"
      "v_add_u32 v73, 0x4000, v79\n\t"
      "v_mov_b32 v74, v80\n\t"
      "v_and_b32 v75, 0x4000, v76\n\t"
      "v_add_u32 v75, v75, v80\n\t"
      "s_mov_b32 s42, 0\n\t"
      "s_mov_b32 s45, 0xc000\n\t"
      "s_mov_b32 s46, 0x8000\n\t"
      "ds_read_b128 v[60:63], v72\n\t"
      "s_waitcnt lgkmcnt(0)\n\t"
      "v_mfma_f32_32x32x16_bf16 v[16:31], v[60:63], v[56:59], 0\n\t"
      "ds_read_b128 v[60:63], v72 offset:1024\n\t"
      "ds_read_b128 v[64:67], v74\n\t"
      "ds_read_b128 v[68:71], v74 offset:512\n\t"
      "s_nop 7\n\t"
      "s_nop 7\n\t"
      "v_exp_f32 v16, v16\n\t"
      "v_exp_f32 v17, v17\n\t"
      "0:\n\t"
      A_TILE_AB("v72 offset:2048", "v74 offset:1024", "v74 offset:1536")
      A_TILE_BA("v72 offset:3072", "v74 offset:2048", "v74 offset:2560")
      A_TILE_AB("v72 offset:4096", "v74 offset:3072", "v74 offset:3584")
      A_TILE_BA("v72 offset:5120", "v74 offset:4096", "v74 offset:4608")
      A_TILE_AB("v72 offset:6144", "v74 offset:5120", "v74 offset:5632")
      A_TILE_BA("v72 offset:7168", "v74 offset:6144", "v74 offset:6656")
      "s_cmp_gt_u32 s42, 4\n\t"
      "s_cbranch_scc1 1f\n\t"
      "s_waitcnt vmcnt(2)\n\t"
      "s_barrier\n\t"
      "s_add_u32 s44, s43, s45\n\t"
      "s_mov_b32 m0, s44\n\t"
      "s_add_u32 s44, s44, 0x2000\n\t"
      "global_load_lds_dwordx4 v77, s[40:41]\n\t"
      "s_mov_b32 m0, s44\n\t"
      "s_add_u32 s45, s45, 0x4000\n\t"
      "s_and_b32 s45, s45, 0xffff\n\t"
      "global_load_lds_dwordx4 v78, s[40:41]\n\t"
      "s_add_u32 s40, s40, 0x2000\n\t"
      "s_addc_u32 s41, s41, 0\n\t"
      "s_branch 2f\n\t"
      "1:\n\t"
      "s_waitcnt vmcnt(0)\n\t"
      "s_barrier\n\t"
      "2:\n\t"
      A_TILE_AB("v73", "v74 offset:7168", "v74 offset:7680")
      A_TILE_BA("v73 offset:1024", "v75", "v75 offset:512")
      "v_mov_b32 v72, v73\n\t"
      "v_mov_b32 v74, v75\n\t"
      "v_add_u32 v73, s46, v79\n\t"
      "v_and_b32 v75, s46, v76\n\t"
      "v_add_u32 v75, v75, v80\n\t"
      "s_add_u32 s46, s46, 0x4000\n\t"
      "s_and_b32 s46, s46, 0xffff\n\t"
      "s_add_u32 s42, s42, 1\n\t"
      "s_cmp_lt_u32 s42, 8\n\t"
      "s_cbranch_scc1 0b\n\t"
      "s_waitcnt lgkmcnt(0)\n\t"
      "s_nop 7\n\t"
      "s_nop 7\n\t"
      : "+{v[0:15]}"(o), "+{s[40:41]}"(kvbase)
      : "{v[56:59]}"(q), "{v79}"(kl), "{v80}"(vl), "{v76}"(vmask), "{v77}"(gk), "{v78}"(gv), "{s43}"(ldst)
      : "v16", "v17", "v18", "v19", "v20", "v21", "v22", "v23", "v24", "v25", "v26", "v27", "v28", "v29", "v30", "v31", "v32", "v33", "v34",
        "v35", "v36", "v37", "v38", "v39", "v40", "v41", "v42", "v43", "v44", "v45", "v46", "v47", "v48", "v49", "v50", "v51", "v52", "v53",
        "v54", "v55", "v60", "v61", "v62", "v63", "v64", "v65", "v66", "v67", "v68", "v69", "v70", "v71", "v72", "v73", "v74", "v75", "s42",
        "s44", "s45", "s46", "m0", "scc", "memory");
}
DEV void role_attn(char* __restrict__ ws, char* sm, int b) {
  const int tid = threadIdx.x, lane = tid & 63, w = __builtin_amdgcn_readfirstlane(tid >> 6);
  const int head = (b & 7) * 4 + (b >> 6), qg = (b >> 3) & 7;
  const char* kg = ws + WS_KF + (size_t)head * 65536 + w * 1024 + lane * 16;
  const char* vg = kg + (WS_VF - WS_KF);
  auto loadKV = [&](int ch, int buf) {
    glds16(kg + ch * 8192, sm + buf * 16384 + w * 1024);
    glds16(vg + ch * 8192, sm + buf * 16384 + 8192 + w * 1024);
  };
  const int qt = qg * 8 + w;
  const float* nr = (const float*)(ws + WS_NR);
  float kmax = nr[(32 + head) * 64 + lane], vmax = nr[(64 + head) * 64 + lane], qmax = nr[head * 64 + qg * 8 + (lane & 7)];
  const u32x4 qf = *(const u32x4*)(ws + WS_QF + (size_t)(head * 64 + qt) * 1024 + lane * 16);
  __builtin_amdgcn_sched_barrier(0);
  loadKV(0, 0); loadKV(1, 1); loadKV(2, 2);
  __builtin_amdgcn_sched_barrier(0);
  {
    const uint32_t one = (tid & 15) == 0 ? 0x3f803f80u : 0u;
    *(u32x4*)(sm + AT_ONES + tid * 16) = u32x4{one, one, one, one};
  }
  kmax = row16_max(kmax); vmax = row16_max(vmax); qmax = row16_max(qmax);
  kmax = fmaxf(fmaxf(rdlane(kmax, 0), rdlane(kmax, 16)), fmaxf(rdlane(kmax, 32), rdlane(kmax, 48)));
  vmax = fmaxf(fmaxf(rdlane(vmax, 0), rdlane(vmax, 16)), fmaxf(rdlane(vmax, 32), rdlane(vmax, 48)));
  const bool fast = __builtin_amdgcn_readfirstlane((int)(qmax * kmax * 1.05f <= FAST_LIMIT && vmax <= 1e15f));
  const int h = lane >> 5, m = lane & 31;
  const int vlane = h * 256 + (m & 15) * 16;
  f32x16 o = {};
  if (fast) {
    asm volatile("s_waitcnt vmcnt(4) lgkmcnt(0)" ::: "memory");
    __builtin_amdgcn_s_barrier();
    const uint32_t sb = ldsaddr(sm);
    attn_fast(o, qf, sb + lane * 16, sb + (m < 16 ? 8192 : AT_ONES) + vlane, m < 16 ? 0xffffffffu : 0u, w * 1024 + lane * 16,
              (uint32_t)(WS_VF - WS_KF) + w * 1024 + lane * 16, (uint64_t)(ws + WS_KF + (size_t)head * 65536 + 3 * 8192), sb + w * 1024);
  } else {
    float mrun = -INFINITY;
    const int vstep = m < 16 ? 1024 : 0;
    const bf16x8 qb = __builtin_bit_cast(bf16x8, qf);
#pragma unroll 1
    for (int ch = 0; ch < 8; ++ch) {
      WAITV(0);
      BAR();
      if (ch >= 1 && ch < 6) loadKV(ch + 2, (ch + 2) & 3);
      const int buf = ch & 3;
      const char* kp = sm + buf * 16384 + lane * 16;
      int va = m < 16 ? buf * 16384 + 8192 + vlane : AT_ONES + vlane;
#pragma unroll 1
      for (int t = 0; t < 8; ++t) {
        f32x16 s = mfma(lds128(kp + t * 1024), qb, f32x16{});
        const bf16x8 v0 = lds128(sm + va), v1 = lds128(sm + va + 512);
        va += vstep;
        float mx = s[0];
#pragma unroll
        for (int j = 1; j < 16; ++j) mx = fmaxf(mx, s[j]);
        mx = fmaxf(half_max(mx), mrun);
        const float al = __builtin_amdgcn_exp2f(mrun - mx);
        mrun = mx;
#pragma unroll
        for (int j = 0; j < 16; ++j) { s[j] = __builtin_amdgcn_exp2f(s[j] - mx); o[j] *= al; }
        o = mfma(v0, __builtin_bit_cast(bf16x8, pk8(s, 0)), o);
        o = mfma(v1, __builtin_bit_cast(bf16x8, pk8(s, 8)), o);
      }
    }
  }
  const float inv = 1.0f / half_sum(o[8]);
#pragma unroll
  for (int j = 0; j < 8; ++j) o[j] *= inv;
  st_sc1(ws + WS_OF + (size_t)(qt * 32 + head) * 1024 + lane * 16, pk8(o, 0));
  signal(ws, 16 + qg * 8 + (head >> 2));
}

constexpr int GU_ST = 24576;
template <int V> struct IC { static constexpr int value = V; };
struct GuF { u32x4 x0, x1; TrF wg, wu; };
template <int I, int K> DEV void gu_read(GuF& f, uint32_t bx, uint32_t bw) {
  rd128<I * GU_ST + K * 1024>(f.x0, bx); rd128<I * GU_ST + K * 1024 + 2048>(f.x1, bx);
  rdtr2<I * GU_ST + K * 4096, I * GU_ST + K * 4096 + 1024>(f.wg, bw);
  rdtr2<I * GU_ST + K * 4096 + 8192, I * GU_ST + K * 4096 + 9216>(f.wu, bw);
}
DEV void ffn_gateup(const uint16_t* __restrict__ w1, const uint16_t* __restrict__ w3, char* __restrict__ ws, char* sm, int fbk) {
  const int tid = threadIdx.x, lane = tid & 63, w = __builtin_amdgcn_readfirstlane(tid >> 6);
  const int xcd = fbk & 7, idx = fbk >> 3;
  const int tg = (xcd & 3) * 4 + (idx & 3), fg = (xcd >> 2) * 7 + (idx >> 2);
  const uint32_t sb = __builtin_amdgcn_readfirstlane(ldsaddr(sm));
  const uint64_t xsrc = sgpr64((uint64_t)(ws + WS_XN + (size_t)((tg * 4 + (w >> 1)) * 32 + (w & 1)) * 1024));
  const int wrow = lane >> 4, wch = (lane & 15) ^ ((wrow & 3) << 2);
  const uint64_t w1s = sgpr64((uint64_t)(w1 + (size_t)(4 * w) * 1792 + fg * 128)), w3s = sgpr64((uint64_t)(w3 + (size_t)(4 * w) * 1792 + fg * 128));
  const uint32_t woff = wrow * 3584 + wch * 16, l16 = lane * 16;
  auto load = [&](int st, int buf) {
    const uint32_t d = sb + buf * GU_ST + w * 1024;
    dma(l16, xsrc + st * 2048, d);
    dma(woff, w1s + (uint64_t)st * (32 * 3584), d + 8192);
    dma(woff, w3s + (uint64_t)st * (32 * 3584), d + 16384);
  };
  load(0, 0); load(1, 1); load(2, 2);
  const int h = lane >> 5, q4 = (lane & 15) >> 2, p4 = lane & 3, blk = (lane >> 4) & 1;
  const int c = w & 3, tp = w >> 2;
  const uint32_t bw = sb + 8192 + (8 * h + q4) * 256 + (((4 * c + 2 * blk + (p4 >> 1)) ^ (q4 << 2)) << 4) + 8 * (p4 & 1);
  const uint32_t bx = sb + tp * 4096 + lane * 16;
  f32x16 g0 = {}, g1 = {}, u0 = {}, u1 = {};
  GuF fa, fb;
  WAITV(6);
  BAR();
  gu_read<0, 0>(fa, bx, bw);
  auto stage = [&](auto slot, auto kind, int st) {
    constexpr int I = decltype(slot)::value, KIND = decltype(kind)::value;
    gu_read<I, 1>(fb, bx, bw);
    asm volatile(LGW(6) : "+v"(fa.x0), "+v"(fa.x1), "+v"(fa.wg.lo), "+v"(fa.wg.hi), "+v"(fa.wu.lo), "+v"(fa.wu.hi));
    g0 = mfma(asb(fa.wg), asb(fa.x0), g0); g1 = mfma(asb(fa.wg), asb(fa.x1), g1);
    u0 = mfma(asb(fa.wu), asb(fa.x0), u0); u1 = mfma(asb(fa.wu), asb(fa.x1), u1);
    __builtin_amdgcn_sched_barrier(0);
    if constexpr (KIND == 2) WAITV(0); else if constexpr (KIND < 2) WAITV(3);
    asm volatile(LGW(0) : "+v"(fb.x0), "+v"(fb.x1), "+v"(fb.wg.lo), "+v"(fb.wg.hi), "+v"(fb.wu.lo), "+v"(fb.wu.hi));
    if constexpr (KIND < 3) {
      __builtin_amdgcn_s_barrier();
      asm volatile("" ::: "memory");
      if constexpr (KIND == 0) load(st + 3, I);
      gu_read<(I + 1) % 3, 0>(fa, bx, bw);
    }
    __builtin_amdgcn_sched_barrier(0);
    g0 = mfma(asb(fb.wg), asb(fb.x0), g0); g1 = mfma(asb(fb.wg), asb(fb.x1), g1);
    u0 = mfma(asb(fb.wu), asb(fb.x0), u0); u1 = mfma(asb(fb.wu), asb(fb.x1), u1);
    __builtin_amdgcn_sched_barrier(0);
  };
#pragma unroll 1
  for (int st0 = 0; st0 < 12; st0 += 3) {
    stage(IC<0>{}, IC<0>{}, st0); stage(IC<1>{}, IC<0>{}, st0 + 1); stage(IC<2>{}, IC<0>{}, st0 + 2);
  }
  stage(IC<0>{}, IC<0>{}, 12); stage(IC<1>{}, IC<1>{}, 13); stage(IC<2>{}, IC<2>{}, 14); stage(IC<0>{}, IC<3>{}, 15);
#pragma unroll
  for (int t = 0; t < 2; ++t) {
    f32x16 gg = t ? g1 : g0, uu = t ? u1 : u0;
#pragma unroll
    for (int j = 0; j < 16; ++j) {
      const float e = __builtin_amdgcn_exp2f(gg[j] * -1.4426950408889634f);
      gg[j] = gg[j] * __builtin_amdgcn_rcpf(1.f + e) * uu[j];
    }
    char* dst = ws + WS_HF + (size_t)((tg * 4 + tp * 2 + t) * 112 + (fg * 4 + c) * 2) * 1024 + lane * 16;
    st_sc1(dst, pk8(gg, 0));
    st_sc1(dst + 1024, pk8(gg, 8));
  }
  signal(ws, tg);
}

DEV void ld_sc1(u32x4& d, uint32_t off, uint64_t base) { asm volatile("global_load_dwordx4 %0, %1, %2 sc1" : "=v"(d) : "v"(off), "s"(base)); }
struct DnF { u32x4 a0, a1; };
template <int S> DEV void dn_mma(f32x16 (&acc)[4], DnF& f, uint32_t wa0, uint32_t wa1) {
  TrF w0, w1;
  rdtr2<S * 2048, S * 2048 + 1024>(w0, wa0); rdtr2<S * 2048, S * 2048 + 1024>(w1, wa1);
  asm volatile(LGW(2) : "+v"(w0.lo), "+v"(w0.hi));
  acc[0] = mfma(asb(f.a0), asb(w0), acc[0]); acc[1] = mfma(asb(f.a1), asb(w0), acc[1]);
  __builtin_amdgcn_sched_barrier(0);
  asm volatile(LGW(0) : "+v"(w1.lo), "+v"(w1.hi));
  acc[2] = mfma(asb(f.a0), asb(w1), acc[2]); acc[3] = mfma(asb(f.a1), asb(w1), acc[3]);
  __builtin_amdgcn_sched_barrier(0);
}
#define RW(j) "ds_write_addtid_b32 %" #j " offset:%c17+" #j "*256\n\t"
DEV void red_write(const f32x16& a, uint32_t base, int c) {
  asm volatile("s_mov_b32 m0, %16\n\ts_nop 0\n\t" RW(0) RW(1) RW(2) RW(3) RW(4) RW(5) RW(6) RW(7) RW(8) RW(9) RW(10) RW(11) RW(12) RW(13) RW(14) RW(15)
               ::"v"(a[0]), "v"(a[1]), "v"(a[2]), "v"(a[3]), "v"(a[4]), "v"(a[5]), "v"(a[6]), "v"(a[7]), "v"(a[8]), "v"(a[9]), "v"(a[10]),
               "v"(a[11]), "v"(a[12]), "v"(a[13]), "v"(a[14]), "v"(a[15]), "s"(base), "i"(c * 4096) : "memory");
}
#undef RW
#define DN_WAIT(n, f) asm volatile("s_waitcnt vmcnt(" #n ")" : "+v"(f.a0), "+v"(f.a1)::"memory")
DEV void ffn_down(const uint16_t* __restrict__ x, const uint16_t* __restrict__ wo, const uint16_t* __restrict__ w2, char* __restrict__ ws,
                  uint16_t* __restrict__ out, char* sm, int fb) {
  const int tid = threadIdx.x, lane = tid & 63, w = __builtin_amdgcn_readfirstlane(tid >> 6);
  const int xcd = fb & 7, idx = fb >> 3;
  const int tg = (xcd & 3) * 8 + (idx & 7), cg = (xcd >> 2) * 4 + (idx >> 3);
  const int wrow = lane >> 3, wch = (lane & 7) ^ (((wrow >> 1) & 1) << 2);
  const uint32_t woff = wrow * 1024 + wch * 16;
  const uint32_t sb = __builtin_amdgcn_readfirstlane(ldsaddr(sm)), ring = sb + w * 8192;
  auto loadW = [&](uint64_t wp, uint32_t dst) {
    dma(woff, wp, dst);
    dma(woff, wp + 8 * 1024, dst + 1024);
  };
  const uint64_t w2p = sgpr64((uint64_t)(w2 + (size_t)(14 * w) * 16 * 512 + cg * 64));
  const uint64_t wop = sgpr64((uint64_t)(wo + (size_t)(4 * w) * 16 * 512 + cg * 64));
  const uint64_t hb = sgpr64((uint64_t)(ws + WS_HF + (size_t)(tg * 2 * 112 + 14 * w) * 1024));
  const uint32_t l16 = lane * 16;
  const int h = lane >> 5, q4 = (lane & 15) >> 2, p4 = lane & 3, blk = (lane >> 4) & 1;
  const uint32_t wl = (4 * h + q4) * 128 + (((2 * blk + (p4 >> 1)) ^ ((q4 >> 1) << 2)) << 4) + 8 * (p4 & 1);
  const uint32_t wa0 = ring + wl, wa1 = wa0 ^ 64;
  f32x16 acc[4] = {};
  DnF f[4];
#pragma unroll
  for (int s = 0; s < 4; ++s) loadW(w2p + s * 16384, ring + s * 2048);
  await(ws, tg >> 1, 14);
#pragma unroll
  for (int s = 0; s < 4; ++s) { ld_sc1(f[s].a0, l16 + s * 1024, hb); ld_sc1(f[s].a1, l16 + s * 1024 + 112 * 1024, hb); }
#define DN_STEP(i, n)                                                                                               \
  { constexpr int s = (i) & 3;                                                                                      \
    DN_WAIT(n, f[s]);                                                                                               \
    dn_mma<s>(acc, f[s], wa0, wa1);                                                                                 \
    if constexpr ((i) + 4 < 14) {                                                                                   \
      loadW(w2p + ((i) + 4) * 16384, ring + s * 2048);                                                                         \
      ld_sc1(f[s].a0, l16 + ((i) + 4) * 1024, hb); ld_sc1(f[s].a1, l16 + ((i) + 4) * 1024 + 112 * 1024, hb);         \
    } else loadW(wop + ((i) - 10) * 16384, ring + s * 2048);                                                        \
  }
  DN_STEP(0, 6) DN_STEP(1, 8) DN_STEP(2, 10) DN_STEP(3, 12)
  DN_STEP(4, 12) DN_STEP(5, 12) DN_STEP(6, 12) DN_STEP(7, 12) DN_STEP(8, 12) DN_STEP(9, 12)
  DN_STEP(10, 12) DN_STEP(11, 10) DN_STEP(12, 8) DN_STEP(13, 6)
#undef DN_STEP
  asm volatile("" : "+v"(acc[0]), "+v"(acc[1]), "+v"(acc[2]), "+v"(acc[3]));
  const int oc = (lane >> 4) & 1, oq = w >> 1, oi = (lane >> 5) | ((w & 1) << 1), orow = 8 * oq + 4 * ((lane >> 3) & 1) + oi;
  const size_t oo = (size_t)(tg * 64 + orow) * 512 + cg * 64 + oc * 32 + (lane & 7) * 4;
  u32x2 xv0, xv1;
  asm volatile("global_load_dwordx2 %0, %1, off" : "=v"(xv0) : "v"(x + oo));
  asm volatile("global_load_dwordx2 %0, %1, off" : "=v"(xv1) : "v"(x + oo + 32 * 512));
  const uint64_t ob = sgpr64((uint64_t)(ws + WS_OF + (size_t)(tg * 64 + 4 * w) * 1024));
  await_wave(sgpr64((uint64_t)(ws + WS_CT + (16 + (tg >> 2) * 8 + w) * 256)), 4);
#pragma unroll
  for (int s = 0; s < 4; ++s) { ld_sc1(f[s].a0, l16 + s * 1024, ob); ld_sc1(f[s].a1, l16 + s * 1024 + 32 * 1024, ob); }
  DN_WAIT(6, f[0]); dn_mma<2>(acc, f[0], wa0, wa1);
  DN_WAIT(4, f[1]); dn_mma<3>(acc, f[1], wa0, wa1);
  DN_WAIT(2, f[2]); dn_mma<0>(acc, f[2], wa0, wa1);
  DN_WAIT(0, f[3]); dn_mma<1>(acc, f[3], wa0, wa1);
  asm volatile("" : "+v"(xv0), "+v"(xv1));
  typedef __attribute__((ext_vector_type(4))) float f32x4;
  const f32x4* red = (const f32x4*)(sm + (oc * 16 + 4 * oq + oi) * 256 + (lane & 15) * 16);
  asm volatile("s_nop 11" : "+v"(acc[0]), "+v"(acc[2]));
  red_write(acc[0], ring, 0); red_write(acc[2], ring, 1);
  asm volatile("" : "+v"(acc[1]), "+v"(acc[3]));
  BAR();
  f32x4 p[8];
#pragma unroll
  for (int k = 0; k < 8; ++k) p[k] = red[k * 512];
  asm volatile("" : "+v"(p[0]), "+v"(p[1]), "+v"(p[2]), "+v"(p[3]), "+v"(p[4]), "+v"(p[5]), "+v"(p[6]), "+v"(p[7]));
  BAR();
  asm volatile("s_nop 11" : "+v"(acc[1]), "+v"(acc[3]));
  red_write(acc[1], ring, 0); red_write(acc[3], ring, 1);
  auto fin = [&](const u32x2& xv, int t) {
    f32x4 s = ((p[0] + p[1]) + (p[2] + p[3])) + ((p[4] + p[5]) + (p[6] + p[7]));
    s += f32x4{bflo(xv[0]), bfhi(xv[0]), bflo(xv[1]), bfhi(xv[1])};
    st_wt2(out + oo + t * 32 * 512, u32x2{pk(s[0], s[1]), pk(s[2], s[3])});
  };
  fin(xv0, 0);
  BAR();
#pragma unroll
  for (int k = 0; k < 8; ++k) p[k] = red[k * 512];
  fin(xv1, 1);
}

__global__ __launch_bounds__(512, 4) void k2_fused(const uint16_t* __restrict__ x, const uint16_t* __restrict__ wo, const uint16_t* __restrict__ w1,
                                                   const uint16_t* __restrict__ w3, const uint16_t* __restrict__ w2, char* __restrict__ ws,
                                                   uint16_t* __restrict__ out) {
  __shared__ __attribute__((aligned(1024))) char sm[81920];
  const int b = blockIdx.x;
  if (b < 256) { role_attn(ws, sm, b); return; }
  const int fb = b - 256;
  __builtin_amdgcn_s_setprio(PRIO);
  __builtin_amdgcn_s_sleep(FDELAY);
  if (fb < 224) ffn_gateup(w1, w3, ws, sm, fb);
  __builtin_amdgcn_s_setprio(PRIO2);
  ffn_down(x, wo, w2, ws, out, sm, fb);
}

extern "C" void kernel_launch(void* const* d_in, const int* in_sizes, int n_in, void* d_out, int out_size, void* d_ws, size_t ws_size,
                              hipStream_t stream) {
  const uint16_t* x = (const uint16_t*)d_in[0];
  const uint16_t* wqkv = (const uint16_t*)d_in[1];
  const uint16_t* wo = (const uint16_t*)d_in[2];
  const uint16_t* w1 = (const uint16_t*)d_in[4];
  const uint16_t* w3 = (const uint16_t*)d_in[5];
  const uint16_t* w2 = (const uint16_t*)d_in[6];
  char* ws = (char*)d_ws;
  k1_qkv<<<256, 576, 0, stream>>>(x, wqkv, ws, wo, w1, w3, w2);
  k2_fused<<<512, 512, 0, stream>>>(x, wo, w1, w3, w2, ws, (uint16_t*)d_out);
}
```
